```python
import torch

def _softmax(x, axis=-1):
    return torch.softmax(x, dim=axis)
def _gelu(x):
    return torch.nn.functional.gelu(x, approximate="tanh")
def _silu(x):
    return torch.nn.functional.silu(x)
def _sigmoid(x):
    return torch.sigmoid(x)
def _layernorm(x, axis=-1, eps=1e-5):
    m = x.mean(dim=axis, keepdim=True)
    v = x.var(dim=axis, keepdim=True, unbiased=False)
    return (x - m) / torch.sqrt(v + eps)
def _scatter_add(tgt, idx, src, axis=0):
    return tgt.clone().index_add_(axis, idx, src)
def _segment_sum(src, seg, n):
    out = torch.zeros((n,) + src.shape[1:], dtype=src.dtype)
    return out.index_add_(0, seg, src)
def _pad(x, pw):
    flat = tuple(v for pair in reversed(pw) for v in pair)
    return torch.nn.functional.pad(x, flat)

kernel_name = 'interleave_layers__spectral_gfnet__dense_activation_gelu__prompt'

def setup_inputs():

    _w = (lambda a: (lambda t: t.to(torch.bfloat16) if t.is_floating_point() else t)(torch.as_tensor(a)))
    def _A_setup():
        B, S, D = 1, 2048, 640
        Df = D // 2 + 1
        return {
            "x": _w((lambda *s: torch.randn(*s, dtype=torch.bfloat16) * 0.05)(B, S, D)),
            "w_r": _w((lambda *s: torch.randn(*s, dtype=torch.bfloat16) * 0.4)(S, Df) * 0.02),
            "w_i": _w((lambda *s: torch.randn(*s, dtype=torch.bfloat16) * 0.4)(S, Df) * 0.02),
        }
    _A_in = _A_setup()

    def _B_setup():
        B, S, D, F = 1, 2048, 640, 1728
        return {
            "x": _w((lambda *s: torch.randn(*s, dtype=torch.bfloat16) * 0.05)(B, S, D)),
            "w1": _w((lambda *s: torch.randn(*s, dtype=torch.bfloat16) * 0.4)(D, F) * (D ** -0.5)),
            "b1": _w((lambda *s: torch.randn(*s, dtype=torch.bfloat16) * 12.8)(F) * 0.01),
            "w2": _w((lambda *s: torch.randn(*s, dtype=torch.bfloat16) * 0.1)(F, D) * (F ** -0.5)),
            "b2": _w((lambda *s: torch.randn(*s, dtype=torch.bfloat16) * 0.8)(D) * 0.01),
        }
    _B_in = _B_setup()

    return {
        "A_x": _A_in["x"],
        "A_w_r": _A_in["w_r"],
        "A_w_i": _A_in["w_i"],
        "B_x": _B_in["x"],
        "B_w1": _B_in["w1"],
        "B_b1": _B_in["b1"],
        "B_w2": _B_in["w2"],
        "B_b2": _B_in["b2"],
    }

def reference(A_x, A_w_r, A_w_i, B_x, B_w1, B_b1, B_w2, B_b2):
    def _A_ref(x, w_r, w_i):
        B, S, D = x.shape
        X = (lambda x, axes=(-2,-1): torch.fft.rfft2(x.float(), dim=axes))(x, axes=(-2, -1))
        W = w_r + 1j * w_i
        X = X * W
        y = (lambda x, s, axes=(-2,-1): torch.fft.irfft2(x, s=s, dim=axes).to(torch.bfloat16))(X, (S, D), axes=(-2, -1))
        return y

    def _B_ref(x, w1, b1, w2, b2):
        h = torch.einsum("bsd,df->bsf", x, w1) + b1
        h = _gelu(h)
        return torch.einsum("bsf,fd->bsd", h, w2) + b2

    x = A_x
    x = x + _A_ref(_layernorm(x, axis=-1), A_w_r, A_w_i)
    x = x + _B_ref(_layernorm(x, axis=-1), B_w1, B_b1, B_w2, B_b2)
    x = x + _A_ref(_layernorm(x, axis=-1), A_w_r, A_w_i)
    x = x + _B_ref(_layernorm(x, axis=-1), B_w1, B_b1, B_w2, B_b2)
    return x
```

```cpp
#include <hip/hip_runtime.h>
#include <hip/hip_bf16.h>
#include <stdint.h>

typedef __attribute__((ext_vector_type(8))) short bf16x8;
typedef __attribute__((ext_vector_type(4))) float f32x4;
typedef unsigned short u16;

constexpr int S_ = 2048, D_ = 640, DF_ = 321, F_ = 1728;

struct alignas(8) cf { float x, y; };
constexpr double kPi = 3.14159265358979323846264338327950288;
constexpr double c_sin(double x) {
  double term = x, sum = x;
  for (int i = 1; i < 26; ++i) { term *= -x * x / (double)((2 * i) * (2 * i + 1)); sum += term; }
  return sum;
}
constexpr double c_cos(double x) {
  double term = 1.0, sum = 1.0;
  for (int i = 1; i < 26; ++i) { term *= -x * x / (double)((2 * i - 1) * (2 * i)); sum += term; }
  return sum;
}
template <int N> struct TwTab {
  cf v[N];
  constexpr TwTab() : v{} {
    for (int i = 0; i < N; ++i) {
      double a = 2.0 * kPi * (double)i / (double)N;
      if (a > kPi) a -= 2.0 * kPi;
      v[i].x = (float)c_cos(a);
      v[i].y = (float)c_sin(a);
    }
  }
};
__device__ constexpr TwTab<2048> g_tw2048{};
__device__ constexpr TwTab<640> g_tw640{};

__device__ __forceinline__ float bf2f(u16 u) { return __uint_as_float(((unsigned)u) << 16); }
__device__ __forceinline__ u16 f2bf(float x) {
  __hip_bfloat16 h = __float2bfloat16(x);
  return *reinterpret_cast<u16*>(&h);
}
__device__ __forceinline__ float bfr(float x) { return bf2f(f2bf(x)); }
__device__ __forceinline__ unsigned pack2(float lo, float hi) {
  return (unsigned)f2bf(lo) | ((unsigned)f2bf(hi) << 16);
}

__device__ __forceinline__ cf cadd(cf a, cf b) { return {a.x + b.x, a.y + b.y}; }
__device__ __forceinline__ cf csub(cf a, cf b) { return {a.x - b.x, a.y - b.y}; }
template <int DIR> __device__ __forceinline__ cf ctw(cf a, cf w) {
  if (DIR > 0) return {a.x * w.x - a.y * w.y, a.x * w.y + a.y * w.x};
  else return {a.x * w.x + a.y * w.y, a.y * w.x - a.x * w.y};
}
template <int DIR> __device__ __forceinline__ cf cmuli(cf a) {
  if (DIR > 0) return {-a.y, a.x};
  else return {a.y, -a.x};
}

template <int DIR> __device__ __forceinline__ void fft4(cf& a0, cf& a1, cf& a2, cf& a3) {
  cf t0 = cadd(a0, a2), t1 = csub(a0, a2), t2 = cadd(a1, a3), t3 = cmuli<DIR>(csub(a1, a3));
  a0 = cadd(t0, t2); a1 = cadd(t1, t3); a2 = csub(t0, t2); a3 = csub(t1, t3);
}
template <int DIR> __device__ __forceinline__ void fft8(cf (&a)[8]) {
  constexpr float R = 0.70710678118654752440f;
  cf e0 = a[0], e1 = a[2], e2 = a[4], e3 = a[6];
  cf o0 = a[1], o1 = a[3], o2 = a[5], o3 = a[7];
  fft4<DIR>(e0, e1, e2, e3);
  fft4<DIR>(o0, o1, o2, o3);
  cf t1, t3;
  if (DIR > 0) { t1 = {(o1.x - o1.y) * R, (o1.x + o1.y) * R}; t3 = {(-o3.x - o3.y) * R, (o3.x - o3.y) * R}; }
  else         { t1 = {(o1.x + o1.y) * R, (o1.y - o1.x) * R}; t3 = {(o3.y - o3.x) * R, (-o3.x - o3.y) * R}; }
  cf t2 = cmuli<DIR>(o2);
  a[0] = cadd(e0, o0); a[4] = csub(e0, o0);
  a[1] = cadd(e1, t1); a[5] = csub(e1, t1);
  a[2] = cadd(e2, t2); a[6] = csub(e2, t2);
  a[3] = cadd(e3, t3); a[7] = csub(e3, t3);
}
template <int DIR> __device__ __forceinline__ void fft16(cf (&x)[16]) {
  constexpr float C1 = 0.92387953251128675613f, S1 = 0.38268343236508977173f, R = 0.70710678118654752440f;
#pragma unroll
  for (int a = 0; a < 4; ++a) fft4<DIR>(x[a], x[a + 4], x[a + 8], x[a + 12]);
  const cf w1 = {C1, S1}, w2 = {R, R}, w3 = {S1, C1}, w4 = {0.f, 1.f}, w6 = {-R, R}, w9 = {-C1, -S1};
  x[1 + 4] = ctw<DIR>(x[1 + 4], w1);  x[1 + 8] = ctw<DIR>(x[1 + 8], w2);  x[1 + 12] = ctw<DIR>(x[1 + 12], w3);
  x[2 + 4] = ctw<DIR>(x[2 + 4], w2);  x[2 + 8] = ctw<DIR>(x[2 + 8], w4);  x[2 + 12] = ctw<DIR>(x[2 + 12], w6);
  x[3 + 4] = ctw<DIR>(x[3 + 4], w3);  x[3 + 8] = ctw<DIR>(x[3 + 8], w6);  x[3 + 12] = ctw<DIR>(x[3 + 12], w9);
#pragma unroll
  for (int r = 0; r < 4; ++r) fft4<DIR>(x[4 * r], x[4 * r + 1], x[4 * r + 2], x[4 * r + 3]);
  cf t;
#define SW(i, j) t = x[i]; x[i] = x[j]; x[j] = t;
  SW(1, 4) SW(2, 8) SW(3, 12) SW(6, 9) SW(7, 13) SW(11, 14)
#undef SW
}
template <int DIR> __device__ __forceinline__ void fft5(cf (&a)[5]) {
  constexpr float c1 = 0.30901699437494742410f, c2 = -0.80901699437494742410f;
  constexpr float s1 = 0.95105651629515357212f, s2 = 0.58778525229247312917f;
  cf t1 = cadd(a[1], a[4]), t2 = cadd(a[2], a[3]), t3 = csub(a[1], a[4]), t4 = csub(a[2], a[3]);
  cf m1 = {a[0].x + c1 * t1.x + c2 * t2.x, a[0].y + c1 * t1.y + c2 * t2.y};
  cf m2 = {a[0].x + c2 * t1.x + c1 * t2.x, a[0].y + c2 * t1.y + c1 * t2.y};
  cf q1 = cmuli<DIR>(cf{s1 * t3.x + s2 * t4.x, s1 * t3.y + s2 * t4.y});
  cf q2 = cmuli<DIR>(cf{s2 * t3.x - s1 * t4.x, s2 * t3.y - s1 * t4.y});
  a[0] = {a[0].x + t1.x + t2.x, a[0].y + t1.y + t2.y};
  a[1] = cadd(m1, q1); a[4] = csub(m1, q1);
  a[2] = cadd(m2, q2); a[3] = csub(m2, q2);
}

__device__ __forceinline__ float wave_sum(float v) {
#pragma unroll
  for (int o = 32; o > 0; o >>= 1) v += __shfl_xor(v, o, 64);
  return v;
}

__device__ __forceinline__ void layernorm10(float (&v)[10]) {
  float s = 0.f;
#pragma unroll
  for (int i = 0; i < 10; ++i) s += v[i];
  const float mean = wave_sum(s) * (1.0f / D_);
  float q = 0.f;
#pragma unroll
  for (int i = 0; i < 10; ++i) { float d = v[i] - mean; q += d * d; }
  const float var = wave_sum(q) * (1.0f / D_);
  const float m = bfr(mean);
  const float den = bfr(sqrtf(bfr(bfr(var) + 1e-5f)));
#pragma unroll
  for (int i = 0; i < 10; ++i) v[i] = bfr(bfr(v[i] - m) / den);
}

__device__ __forceinline__ void prep_Wp(int idx, const u16* wr, const u16* wi, uint4* Wp) {
  if (idx >= DF_ * 256) return;
  const int k = idx % DF_, t = idx / DF_;
  const int p = t >> 6, r = (t >> 3) & 7, u = t & 7;
  const int base = p + 4 * r + 32 * u;
  unsigned o[8];
#pragma unroll
  for (int v = 0; v < 8; ++v) {
    const int ks = base + 256 * v;
    o[v] = (unsigned)wr[ks * DF_ + k] | ((unsigned)wi[ks * DF_ + k] << 16);
  }
  uint4* dst = Wp + (size_t)(k * 256 + t) * 2;
  dst[0] = make_uint4(o[0], o[1], o[2], o[3]);
  dst[1] = make_uint4(o[4], o[5], o[6], o[7]);
}
__device__ __forceinline__ void prep_frag(int idx, const u16* src, uint4* dst, int K, int N) {
  const int KT = K >> 5;
  if (idx >= (N >> 4) * KT * 64) return;
  const int lane = idx & 63, kt = (idx >> 6) % KT, nt = (idx >> 6) / KT;
  const int n = nt * 16 + (lane & 15), k0 = kt * 32 + (lane >> 4) * 8;
  unsigned o[4];
#pragma unroll
  for (int j = 0; j < 4; ++j)
    o[j] = (unsigned)src[(size_t)(k0 + 2 * j) * N + n] | ((unsigned)src[(size_t)(k0 + 2 * j + 1) * N + n] << 16);
  dst[idx] = make_uint4(o[0], o[1], o[2], o[3]);
}

constexpr int YS = 144;
constexpr int ZS = 18;
__global__ __launch_bounds__(320) void k_row_fwd(const u16* __restrict__ x, float4* __restrict__ T,
                                                 const u16* __restrict__ wr, const u16* __restrict__ wi,
                                                 uint4* __restrict__ Wp) {
  if (blockIdx.x >= S_ / 8) {
    if (Wp) prep_Wp((blockIdx.x - S_ / 8) * 320 + threadIdx.x, wr, wi, Wp);
    return;
  }
  __shared__ cf bufA[4 * 5 * YS];
  __shared__ cf bufB[4 * 40 * ZS];
  const int t = threadIdx.x, wv = t >> 6, lane = t & 63;
  const int row0 = blockIdx.x * 8;
  if (wv < 4) {
    float a[10], b[10];
    const unsigned* pa = reinterpret_cast<const unsigned*>(x + (size_t)(row0 + 2 * wv) * D_) + lane;
    const unsigned* pb = pa + D_ / 2;
#pragma unroll
    for (int i = 0; i < 5; ++i) {
      unsigned ua = pa[64 * i], ub = pb[64 * i];
      a[2 * i] = bf2f((u16)(ua & 0xffff)); a[2 * i + 1] = bf2f((u16)(ua >> 16));
      b[2 * i] = bf2f((u16)(ub & 0xffff)); b[2 * i + 1] = bf2f((u16)(ub >> 16));
    }
    layernorm10(a);
    layernorm10(b);
    cf z0[5], z1[5];
#pragma unroll
    for (int i = 0; i < 5; ++i) { z0[i] = {a[2 * i], b[2 * i]}; z1[i] = {a[2 * i + 1], b[2 * i + 1]}; }
    fft5<-1>(z0);
    fft5<-1>(z1);
    const int n0 = 2 * lane;
    cf* Y = bufA + wv * 5 * YS + n0;
#pragma unroll
    for (int p = 0; p < 5; ++p) {
      cf u0 = z0[p], u1 = z1[p];
      if (p) { u0 = ctw<-1>(u0, g_tw640.v[n0 * p]); u1 = ctw<-1>(u1, g_tw640.v[(n0 + 1) * p]); }
      *reinterpret_cast<float4*>(Y + p * YS) = make_float4(u0.x, u0.y, u1.x, u1.y);
    }
  }
  __syncthreads();
  {
    const int f = t / 80, rem = t % 80, p = rem >> 4, a = rem & 15;
    cf v[8];
    const cf* Y = bufA + (f * 5 + p) * YS + a;
#pragma unroll
    for (int b = 0; b < 8; ++b) v[b] = Y[16 * b];
    fft8<-1>(v);
    cf* Z = bufB + (f * 40 + p) * ZS + a;
#pragma unroll
    for (int r = 0; r < 8; ++r) {
      cf u = v[r];
      if (r) u = ctw<-1>(u, g_tw640.v[5 * a * r]);
      Z[r * 5 * ZS] = u;
    }
  }
  __syncthreads();
  if (t < 160) {
    const int f = t / 40, rem = t % 40;
    cf v[16];
    const float4* Z = reinterpret_cast<const float4*>(bufB + (f * 40 + rem) * ZS);
#pragma unroll
    for (int a = 0; a < 8; ++a) { float4 q = Z[a]; v[2 * a] = {q.x, q.y}; v[2 * a + 1] = {q.z, q.w}; }
    fft16<-1>(v);
    cf* X = bufA + f * 640 + rem;
#pragma unroll
    for (int tt = 0; tt < 16; ++tt) X[40 * tt] = v[tt];
  }
  __syncthreads();
  for (int idx = t; idx < DF_ * 4; idx += 320) {
    const int k = idx >> 2, f = idx & 3;
    const cf zk = bufA[f * 640 + k], zn = bufA[f * 640 + (k ? 640 - k : 0)];
    float4 o;
    o.x = 0.5f * (zk.x + zn.x); o.y = 0.5f * (zk.y - zn.y);
    o.z = 0.5f * (zk.y + zn.y); o.w = 0.5f * (zn.x - zk.x);
    T[((size_t)k * S_ + row0) / 2 + f] = o;
  }
}

constexpr int PR = 576;
__global__ __launch_bounds__(256) void k_col(float4* __restrict__ T, const uint4* __restrict__ Wp,
                                             const u16* __restrict__ wsrc, uint4* __restrict__ wdst) {
  if (blockIdx.x >= DF_) {
    if (wdst) prep_frag((blockIdx.x - DF_) * 256 + threadIdx.x, wsrc, wdst, D_, F_);
    return;
  }
  __shared__ cf buf[4 * PR];
  const int t = threadIdx.x, wv = t >> 6, lane = t & 63;
  const int col = blockIdx.x;
  float4* Tc = T + (size_t)col * (S_ / 2);
  const uint4 wq0 = Wp[(size_t)(col * 256 + t) * 2], wq1 = Wp[(size_t)(col * 256 + t) * 2 + 1];
  cf tw1a[3], tw1b[3], tw2[7], tw3[7];
  const int n0 = 2 * t;
  const int c3 = lane & 7;
  {
    cf z0[4], z1[4];
#pragma unroll
    for (int i = 0; i < 4; ++i) { float4 q = Tc[t + 256 * i]; z0[i] = {q.x, q.y}; z1[i] = {q.z, q.w}; }
#pragma unroll
    for (int p = 1; p < 4; ++p) { tw1a[p - 1] = g_tw2048.v[n0 * p]; tw1b[p - 1] = g_tw2048.v[(n0 + 1) * p]; }
#pragma unroll
    for (int r = 1; r < 8; ++r) { tw2[r - 1] = g_tw2048.v[4 * lane * r]; tw3[r - 1] = g_tw2048.v[32 * c3 * r]; }
    fft4<-1>(z0[0], z0[1], z0[2], z0[3]);
    fft4<-1>(z1[0], z1[1], z1[2], z1[3]);
#pragma unroll
    for (int p = 0; p < 4; ++p) {
      cf u0 = z0[p], u1 = z1[p];
      if (p) { u0 = ctw<-1>(u0, tw1a[p - 1]); u1 = ctw<-1>(u1, tw1b[p - 1]); }
      *reinterpret_cast<float4*>(buf + p * PR + n0) = make_float4(u0.x, u0.y, u1.x, u1.y);
    }
  }
  __syncthreads();
  cf* R = buf + wv * PR;
  cf v[8];
#pragma unroll
  for (int b = 0; b < 8; ++b) v[b] = R[lane + 64 * b];
  fft8<-1>(v);
  __builtin_amdgcn_fence(__ATOMIC_SEQ_CST, "wavefront");
#pragma unroll
  for (int r = 0; r < 8; ++r) R[r * 72 + lane] = r ? ctw<-1>(v[r], tw2[r - 1]) : v[r];
  __builtin_amdgcn_fence(__ATOMIC_SEQ_CST, "wavefront");
  const int r3 = lane >> 3;
#pragma unroll
  for (int e = 0; e < 8; ++e) v[e] = R[r3 * 72 + c3 + 8 * e];
  fft8<-1>(v);
  __builtin_amdgcn_fence(__ATOMIC_SEQ_CST, "wavefront");
#pragma unroll
  for (int u = 0; u < 8; ++u) R[r3 * 72 + c3 * 9 + u] = u ? ctw<-1>(v[u], tw3[u - 1]) : v[u];
  __builtin_amdgcn_fence(__ATOMIC_SEQ_CST, "wavefront");
#pragma unroll
  for (int c = 0; c < 8; ++c) v[c] = R[r3 * 72 + c * 9 + c3];
  fft8<-1>(v);
  {
    const unsigned w[8] = {wq0.x, wq0.y, wq0.z, wq0.w, wq1.x, wq1.y, wq1.z, wq1.w};
    constexpr float sc = 1.0f / ((float)S_ * (float)D_);
#pragma unroll
    for (int i = 0; i < 8; ++i) {
      const float a = bf2f((u16)(w[i] & 0xffff)), b = bf2f((u16)(w[i] >> 16));
      const cf q = v[i];
      v[i] = {(q.x * a - q.y * b) * sc, (q.x * b + q.y * a) * sc};
    }
  }
  fft8<1>(v);
  __builtin_amdgcn_fence(__ATOMIC_SEQ_CST, "wavefront");
#pragma unroll
  for (int c = 0; c < 8; ++c) R[r3 * 72 + c * 9 + c3] = v[c];
  __builtin_amdgcn_fence(__ATOMIC_SEQ_CST, "wavefront");
#pragma unroll
  for (int u = 0; u < 8; ++u) { cf q = R[r3 * 72 + c3 * 9 + u]; v[u] = u ? ctw<1>(q, tw3[u - 1]) : q; }
  fft8<1>(v);
  __builtin_amdgcn_fence(__ATOMIC_SEQ_CST, "wavefront");
#pragma unroll
  for (int e = 0; e < 8; ++e) R[r3 * 72 + c3 + 8 * e] = v[e];
  __builtin_amdgcn_fence(__ATOMIC_SEQ_CST, "wavefront");
#pragma unroll
  for (int r = 0; r < 8; ++r) { cf q = R[r * 72 + lane]; v[r] = r ? ctw<1>(q, tw2[r - 1]) : q; }
  fft8<1>(v);
  __builtin_amdgcn_fence(__ATOMIC_SEQ_CST, "wavefront");
#pragma unroll
  for (int b = 0; b < 8; ++b) R[lane + 64 * b] = v[b];
  __syncthreads();
  {
    cf z0[4], z1[4];
#pragma unroll
    for (int p = 0; p < 4; ++p) {
      float4 q = *reinterpret_cast<const float4*>(buf + p * PR + n0);
      cf u0 = {q.x, q.y}, u1 = {q.z, q.w};
      if (p) { u0 = ctw<1>(u0, tw1a[p - 1]); u1 = ctw<1>(u1, tw1b[p - 1]); }
      z0[p] = u0; z1[p] = u1;
    }
    fft4<1>(z0[0], z0[1], z0[2], z0[3]);
    fft4<1>(z1[0], z1[1], z1[2], z1[3]);
#pragma unroll
    for (int i = 0; i < 4; ++i) Tc[t + 256 * i] = make_float4(z0[i].x, z0[i].y, z1[i].x, z1[i].y);
  }
}

__global__ __launch_bounds__(320) void k_row_inv(const float4* __restrict__ T, const u16* __restrict__ x,
                                                 u16* __restrict__ x1, u16* __restrict__ uf,
                                                 const u16* __restrict__ wsrc, uint4* __restrict__ wdst) {
  if (blockIdx.x >= S_ / 8) {
    if (wdst) prep_frag((blockIdx.x - S_ / 8) * 320 + threadIdx.x, wsrc, wdst, F_, D_);
    return;
  }
  __shared__ cf bufA[4 * 5 * YS];
  __shared__ cf bufB[4 * 40 * ZS];
  const int t = threadIdx.x, wv = t >> 6, lane = t & 63;
  const int row0 = blockIdx.x * 8;
  unsigned ra[5], rb[5];
  if (wv < 4) {
    const unsigned* pa = reinterpret_cast<const unsigned*>(x + (size_t)(row0 + 2 * wv) * D_) + lane;
    const unsigned* pb = pa + D_ / 2;
#pragma unroll
    for (int i = 0; i < 5; ++i) { ra[i] = pa[64 * i]; rb[i] = pb[64 * i]; }
  }
  for (int idx = t; idx < DF_ * 4; idx += 320) {
    const int k = idx >> 2, f = idx & 3;
    const float4 q = T[((size_t)k * S_ + row0) / 2 + f];
    cf* X = bufA + f * 640;
    if (k == 0 || k == 320) {
      X[k] = {q.x, q.z};
    } else {
      X[k] = {q.x - q.w, q.y + q.z};
      X[640 - k] = {q.x + q.w, q.z - q.y};
    }
  }
  __syncthreads();
  if (t < 160) {
    const int f = t / 40, rem = t % 40, r = rem / 5;
    cf v[16];
    const cf* X = bufA + f * 640 + rem;
#pragma unroll
    for (int tt = 0; tt < 16; ++tt) v[tt] = X[40 * tt];
    fft16<1>(v);
    float4* Z = reinterpret_cast<float4*>(bufB + (f * 40 + rem) * ZS);
#pragma unroll
    for (int a = 0; a < 8; ++a) {
      cf u0 = ctw<1>(v[2 * a], g_tw640.v[5 * (2 * a) * r]), u1 = ctw<1>(v[2 * a + 1], g_tw640.v[5 * (2 * a + 1) * r]);
      Z[a] = make_float4(u0.x, u0.y, u1.x, u1.y);
    }
  }
  __syncthreads();
  {
    const int f = t / 80, rem = t % 80, p = rem >> 4, a = rem & 15;
    cf v[8];
    const cf* Z = bufB + (f * 40 + p) * ZS + a;
#pragma unroll
    for (int r = 0; r < 8; ++r) v[r] = Z[r * 5 * ZS];
    fft8<1>(v);
    cf* Y = bufA + (f * 5 + p) * YS + a;
#pragma unroll
    for (int b = 0; b < 8; ++b) Y[16 * b] = v[b];
  }
  __syncthreads();
  if (wv < 4) {
    const int n0 = 2 * lane;
    const cf* Y = bufA + wv * 5 * YS + n0;
    cf z0[5], z1[5];
#pragma unroll
    for (int p = 0; p < 5; ++p) {
      float4 q = *reinterpret_cast<const float4*>(Y + p * YS);
      cf u0 = {q.x, q.y}, u1 = {q.z, q.w};
      if (p) { u0 = ctw<1>(u0, g_tw640.v[n0 * p]); u1 = ctw<1>(u1, g_tw640.v[(n0 + 1) * p]); }
      z0[p] = u0; z1[p] = u1;
    }
    fft5<1>(z0);
    fft5<1>(z1);
    float a[10], b[10];
#pragma unroll
    for (int i = 0; i < 5; ++i) {
      a[2 * i] = bfr(bf2f((u16)(ra[i] & 0xffff)) + bfr(z0[i].x));
      a[2 * i + 1] = bfr(bf2f((u16)(ra[i] >> 16)) + bfr(z1[i].x));
      b[2 * i] = bfr(bf2f((u16)(rb[i] & 0xffff)) + bfr(z0[i].y));
      b[2 * i + 1] = bfr(bf2f((u16)(rb[i] >> 16)) + bfr(z1[i].y));
    }
    const int rowa = row0 + 2 * wv;
    unsigned* oa = reinterpret_cast<unsigned*>(x1 + (size_t)rowa * D_) + lane;
    unsigned* ob = oa + D_ / 2;
#pragma unroll
    for (int i = 0; i < 5; ++i) { oa[64 * i] = pack2(a[2 * i], a[2 * i + 1]); ob[64 * i] = pack2(b[2 * i], b[2 * i + 1]); }
    layernorm10(a);
    layernorm10(b);
#pragma unroll
    for (int i = 0; i < 5; ++i) {
      const int d = 128 * i + n0;
      const size_t off = ((size_t)((rowa >> 4) * (D_ / 32) + (d >> 5)) * 64 + ((d >> 3) & 3) * 16 + (rowa & 15)) * 8 + (d & 7);
      *reinterpret_cast<unsigned*>(uf + off) = pack2(a[2 * i], a[2 * i + 1]);
      *reinterpret_cast<unsigned*>(uf + off + 8) = pack2(b[2 * i], b[2 * i + 1]);
    }
  }
}

__device__ __forceinline__ float gelu_tanh(float x) {
  const float y = 0.79788456080286535588f * (x + 0.044715f * x * x * x);
  const float e = __expf(2.0f * y);
  const float th = 1.0f - 2.0f / (e + 1.0f);
  return 0.5f * x * (1.0f + th);
}

__global__ __launch_bounds__(256) void k_gemm1(const bf16x8* __restrict__ w1f, const bf16x8* __restrict__ uf,
                                               const u16* __restrict__ b1, u16* __restrict__ hf) {
  constexpr int KT = D_ / 32;
  const int bid = blockIdx.x;
  const int xcd = bid & 7, loc = bid >> 3;
  const int mt = xcd * 2 + (loc & 1), ft = loc >> 1;
  const int wv = threadIdx.x >> 6, lane = threadIdx.x & 63;
  const int f0 = ft * 128 + (wv >> 1) * 64, m0 = mt * 128 + (wv & 1) * 64;
  if (f0 >= F_) return;
  const bf16x8* pa = w1f + (size_t)(f0 >> 4) * KT * 64 + lane;
  const bf16x8* pb = uf + (size_t)(m0 >> 4) * KT * 64 + lane;
  f32x4 acc[4][4];
#pragma unroll
  for (int i = 0; i < 4; ++i)
#pragma unroll
    for (int j = 0; j < 4; ++j) acc[i][j] = f32x4{0.f, 0.f, 0.f, 0.f};
  constexpr int ST = 3;
  bf16x8 A[ST][4], B[ST][4];
#pragma unroll
  for (int s = 0; s < ST - 1; ++s)
#pragma unroll
    for (int i = 0; i < 4; ++i) { A[s][i] = pa[(i * KT + s) * 64]; B[s][i] = pb[(i * KT + s) * 64]; }
#pragma unroll
  for (int kt = 0; kt < KT; ++kt) {
    const int nx = kt + ST - 1;
    if (nx < KT) {
#pragma unroll
      for (int i = 0; i < 4; ++i) { A[nx % ST][i] = pa[(i * KT + nx) * 64]; B[nx % ST][i] = pb[(i * KT + nx) * 64]; }
    }
#pragma unroll
    for (int i = 0; i < 4; ++i)
#pragma unroll
      for (int j = 0; j < 4; ++j)
        acc[i][j] = __builtin_amdgcn_mfma_f32_16x16x32_bf16(A[kt % ST][i], B[kt % ST][j], acc[i][j], 0, 0, 0);
  }
#pragma unroll
  for (int i = 0; i < 4; ++i) {
    const int f = f0 + 16 * i + (lane >> 4) * 4;
    const uint2 bb = *reinterpret_cast<const uint2*>(b1 + f);
    const float bv[4] = {bf2f((u16)(bb.x & 0xffff)), bf2f((u16)(bb.x >> 16)), bf2f((u16)(bb.y & 0xffff)), bf2f((u16)(bb.y >> 16))};
#pragma unroll
    for (int j = 0; j < 4; ++j) {
      const int m = m0 + 16 * j + (lane & 15);
      float h[4];
#pragma unroll
      for (int r = 0; r < 4; ++r) h[r] = gelu_tanh(bfr(bfr(acc[i][j][r]) + bv[r]));
      const size_t off = ((size_t)((m >> 4) * (F_ / 32) + (f >> 5)) * 64 + ((f >> 3) & 3) * 16 + (m & 15)) * 8 + (f & 7);
      *reinterpret_cast<uint2*>(hf + off) = make_uint2(pack2(h[0], h[1]), pack2(h[2], h[3]));
    }
  }
}

__global__ __launch_bounds__(384) void k_gemm2(const bf16x8* __restrict__ w2f, const bf16x8* __restrict__ hf,
                                               const u16* __restrict__ b2, const u16* __restrict__ x1,
                                               u16* __restrict__ x2) {
  constexpr int KT = F_ / 32;
  constexpr int KW = 18;
  __shared__ f32x4 red[2][2][10][64];
  const int bid = blockIdx.x;
  const int xcd = bid & 7, loc = bid >> 3;
  const int mt = xcd * 4 + (loc & 3), nt = loc >> 2;
  const int wv = threadIdx.x >> 6, lane = threadIdx.x & 63;
  const int wm = wv & 1, wk = wv >> 1;
  const int n0 = nt * 80, m0 = mt * 64 + wm * 32;
  const bf16x8* pa = w2f + ((size_t)(n0 >> 4) * KT + wk * KW) * 64 + lane;
  const bf16x8* pb = hf + ((size_t)(m0 >> 4) * KT + wk * KW) * 64 + lane;
  f32x4 acc[5][2];
#pragma unroll
  for (int i = 0; i < 5; ++i) { acc[i][0] = f32x4{0.f, 0.f, 0.f, 0.f}; acc[i][1] = f32x4{0.f, 0.f, 0.f, 0.f}; }
  constexpr int ST = 4;
  bf16x8 A[ST][5], B[ST][2];
#pragma unroll
  for (int s = 0; s < ST - 1; ++s) {
#pragma unroll
    for (int i = 0; i < 5; ++i) A[s][i] = pa[(i * KT + s) * 64];
    B[s][0] = pb[s * 64]; B[s][1] = pb[(KT + s) * 64];
  }
#pragma unroll
  for (int kt = 0; kt < KW; ++kt) {
    const int nx = kt + ST - 1;
    if (nx < KW) {
#pragma unroll
      for (int i = 0; i < 5; ++i) A[nx % ST][i] = pa[(i * KT + nx) * 64];
      B[nx % ST][0] = pb[nx * 64]; B[nx % ST][1] = pb[(KT + nx) * 64];
    }
#pragma unroll
    for (int i = 0; i < 5; ++i)
#pragma unroll
      for (int j = 0; j < 2; ++j)
        acc[i][j] = __builtin_amdgcn_mfma_f32_16x16x32_bf16(A[kt % ST][i], B[kt % ST][j], acc[i][j], 0, 0, 0);
  }
  if (wk > 0) {
#pragma unroll
    for (int i = 0; i < 5; ++i) { red[wk - 1][wm][i * 2][lane] = acc[i][0]; red[wk - 1][wm][i * 2 + 1][lane] = acc[i][1]; }
  }
  __syncthreads();
  if (wk == 0) {
#pragma unroll
    for (int i = 0; i < 5; ++i) {
      const int n = n0 + 16 * i + (lane >> 4) * 4;
      const uint2 bb = *reinterpret_cast<const uint2*>(b2 + n);
      const float bv[4] = {bf2f((u16)(bb.x & 0xffff)), bf2f((u16)(bb.x >> 16)), bf2f((u16)(bb.y & 0xffff)), bf2f((u16)(bb.y >> 16))};
#pragma unroll
      for (int j = 0; j < 2; ++j) {
        const int m = m0 + 16 * j + (lane & 15);
        const f32x4 s = acc[i][j] + red[0][wm][i * 2 + j][lane] + red[1][wm][i * 2 + j][lane];
        const uint2 xr = *reinterpret_cast<const uint2*>(x1 + (size_t)m * D_ + n);
        const float xv[4] = {bf2f((u16)(xr.x & 0xffff)), bf2f((u16)(xr.x >> 16)), bf2f((u16)(xr.y & 0xffff)), bf2f((u16)(xr.y >> 16))};
        float o[4];
#pragma unroll
        for (int r = 0; r < 4; ++r) o[r] = xv[r] + bfr(bfr(s[r]) + bv[r]);
        *reinterpret_cast<uint2*>(x2 + (size_t)m * D_ + n) = make_uint2(pack2(o[0], o[1]), pack2(o[2], o[3]));
      }
    }
  }
}

extern "C" void kernel_launch(void* const* d_in, const int* in_sizes, int n_in,
                              void* d_out, int out_size, void* d_ws, size_t ws_size,
                              hipStream_t stream) {
  const u16* x0 = (const u16*)d_in[0];
  const u16* wr = (const u16*)d_in[1];
  const u16* wi = (const u16*)d_in[2];
  const u16* w1 = (const u16*)d_in[4];
  const u16* b1 = (const u16*)d_in[5];
  const u16* w2 = (const u16*)d_in[6];
  const u16* b2 = (const u16*)d_in[7];
  u16* out = (u16*)d_out;

  char* ws = (char*)d_ws;
  size_t off = 0;
  auto carve = [&](size_t bytes) { char* p = ws + off; off += (bytes + 255) & ~(size_t)255; return p; };
  float4* T = (float4*)carve((size_t)DF_ * S_ * 8);
  uint4* Wp = (uint4*)carve((size_t)DF_ * 256 * 32);
  uint4* w1f = (uint4*)carve((size_t)D_ * F_ * 2);
  uint4* w2f = (uint4*)carve((size_t)D_ * F_ * 2);
  u16* xa = (u16*)carve((size_t)S_ * D_ * 2);
  u16* xb = (u16*)carve((size_t)S_ * D_ * 2);
  u16* uf = (u16*)carve((size_t)S_ * D_ * 2);
  u16* hf = (u16*)carve((size_t)S_ * F_ * 2);

  const int nWp = (DF_ * 256 + 319) / 320;
  const int nW1 = (D_ * F_ / 8 + 255) / 256;
  const int nW2 = (D_ * F_ / 8 + 319) / 320;

  k_row_fwd<<<S_ / 8 + nWp, 320, 0, stream>>>(x0, T, wr, wi, Wp);
  k_col<<<DF_ + nW1, 256, 0, stream>>>(T, Wp, w1, w1f);
  k_row_inv<<<S_ / 8 + nW2, 320, 0, stream>>>(T, x0, xa, uf, w2, w2f);
  k_gemm1<<<224, 256, 0, stream>>>((const bf16x8*)w1f, (const bf16x8*)uf, b1, hf);
  k_gemm2<<<256, 384, 0, stream>>>((const bf16x8*)w2f, (const bf16x8*)hf, b2, xa, xb);
  k_row_fwd<<<S_ / 8, 320, 0, stream>>>(xb, T, wr, wi, nullptr);
  k_col<<<DF_, 256, 0, stream>>>(T, Wp, w1, nullptr);
  k_row_inv<<<S_ / 8, 320, 0, stream>>>(T, xb, xa, uf, w2, nullptr);
  k_gemm1<<<224, 256, 0, stream>>>((const bf16x8*)w1f, (const bf16x8*)uf, b1, hf);
  k_gemm2<<<256, 384, 0, stream>>>((const bf16x8*)w2f, (const bf16x8*)hf, b2, xa, out);
}
```

```cpp
#include <hip/hip_runtime.h>
#include <hip/hip_bf16.h>
#include <stdint.h>

typedef __attribute__((ext_vector_type(8))) short bf16x8;
typedef __attribute__((ext_vector_type(4))) float f32x4;
typedef unsigned short u16;

constexpr int S_ = 2048, D_ = 640, DF_ = 321, F_ = 1728;

struct alignas(8) cf { float x, y; };
constexpr double kPi = 3.14159265358979323846264338327950288;
constexpr double c_sin(double x) {
  double term = x, sum = x;
  for (int i = 1; i < 26; ++i) { term *= -x * x / (double)((2 * i) * (2 * i + 1)); sum += term; }
  return sum;
}
constexpr double c_cos(double x) {
  double term = 1.0, sum = 1.0;
  for (int i = 1; i < 26; ++i) { term *= -x * x / (double)((2 * i - 1) * (2 * i)); sum += term; }
  return sum;
}
template <int N> struct TwTab {
  cf v[N];
  constexpr TwTab() : v{} {
    for (int i = 0; i < N; ++i) {
      double a = 2.0 * kPi * (double)i / (double)N;
      if (a > kPi) a -= 2.0 * kPi;
      v[i].x = (float)c_cos(a);
      v[i].y = (float)c_sin(a);
    }
  }
};
__device__ constexpr TwTab<2048> g_tw2048{};
__device__ constexpr TwTab<640> g_tw640{};

__device__ __forceinline__ float bf2f(u16 u) { return __uint_as_float(((unsigned)u) << 16); }
__device__ __forceinline__ u16 f2bf(float x) {
  __hip_bfloat16 h = __float2bfloat16(x);
  return *reinterpret_cast<u16*>(&h);
}
__device__ __forceinline__ float bfr(float x) { return bf2f(f2bf(x)); }
__device__ __forceinline__ unsigned pack2(float lo, float hi) {
  return (unsigned)f2bf(lo) | ((unsigned)f2bf(hi) << 16);
}

__device__ __forceinline__ cf cadd(cf a, cf b) { return {a.x + b.x, a.y + b.y}; }
__device__ __forceinline__ cf csub(cf a, cf b) { return {a.x - b.x, a.y - b.y}; }
template <int DIR> __device__ __forceinline__ cf ctw(cf a, cf w) {
  if (DIR > 0) return {a.x * w.x - a.y * w.y, a.x * w.y + a.y * w.x};
  else return {a.x * w.x + a.y * w.y, a.y * w.x - a.x * w.y};
}
template <int DIR> __device__ __forceinline__ cf cmuli(cf a) {
  if (DIR > 0) return {-a.y, a.x};
  else return {a.y, -a.x};
}

template <int DIR> __device__ __forceinline__ void fft4(cf& a0, cf& a1, cf& a2, cf& a3) {
  cf t0 = cadd(a0, a2), t1 = csub(a0, a2), t2 = cadd(a1, a3), t3 = cmuli<DIR>(csub(a1, a3));
  a0 = cadd(t0, t2); a1 = cadd(t1, t3); a2 = csub(t0, t2); a3 = csub(t1, t3);
}
template <int DIR> __device__ __forceinline__ void fft8(cf (&a)[8]) {
  constexpr float R = 0.70710678118654752440f;
  cf e0 = a[0], e1 = a[2], e2 = a[4], e3 = a[6];
  cf o0 = a[1], o1 = a[3], o2 = a[5], o3 = a[7];
  fft4<DIR>(e0, e1, e2, e3);
  fft4<DIR>(o0, o1, o2, o3);
  cf t1, t3;
  if (DIR > 0) { t1 = {(o1.x - o1.y) * R, (o1.x + o1.y) * R}; t3 = {(-o3.x - o3.y) * R, (o3.x - o3.y) * R}; }
  else         { t1 = {(o1.x + o1.y) * R, (o1.y - o1.x) * R}; t3 = {(o3.y - o3.x) * R, (-o3.x - o3.y) * R}; }
  cf t2 = cmuli<DIR>(o2);
  a[0] = cadd(e0, o0); a[4] = csub(e0, o0);
  a[1] = cadd(e1, t1); a[5] = csub(e1, t1);
  a[2] = cadd(e2, t2); a[6] = csub(e2, t2);
  a[3] = cadd(e3, t3); a[7] = csub(e3, t3);
}
template <int DIR> __device__ __forceinline__ void fft16(cf (&x)[16]) {
  constexpr float C1 = 0.92387953251128675613f, S1 = 0.38268343236508977173f, R = 0.70710678118654752440f;
#pragma unroll
  for (int a = 0; a < 4; ++a) fft4<DIR>(x[a], x[a + 4], x[a + 8], x[a + 12]);
  const cf w1 = {C1, S1}, w2 = {R, R}, w3 = {S1, C1}, w4 = {0.f, 1.f}, w6 = {-R, R}, w9 = {-C1, -S1};
  x[1 + 4] = ctw<DIR>(x[1 + 4], w1);  x[1 + 8] = ctw<DIR>(x[1 + 8], w2);  x[1 + 12] = ctw<DIR>(x[1 + 12], w3);
  x[2 + 4] = ctw<DIR>(x[2 + 4], w2);  x[2 + 8] = ctw<DIR>(x[2 + 8], w4);  x[2 + 12] = ctw<DIR>(x[2 + 12], w6);
  x[3 + 4] = ctw<DIR>(x[3 + 4], w3);  x[3 + 8] = ctw<DIR>(x[3 + 8], w6);  x[3 + 12] = ctw<DIR>(x[3 + 12], w9);
#pragma unroll
  for (int r = 0; r < 4; ++r) fft4<DIR>(x[4 * r], x[4 * r + 1], x[4 * r + 2], x[4 * r + 3]);
  cf t;
#define SW(i, j) t = x[i]; x[i] = x[j]; x[j] = t;
  SW(1, 4) SW(2, 8) SW(3, 12) SW(6, 9) SW(7, 13) SW(11, 14)
#undef SW
}
template <int DIR> __device__ __forceinline__ void fft5(cf (&a)[5]) {
  constexpr float c1 = 0.30901699437494742410f, c2 = -0.80901699437494742410f;
  constexpr float s1 = 0.95105651629515357212f, s2 = 0.58778525229247312917f;
  cf t1 = cadd(a[1], a[4]), t2 = cadd(a[2], a[3]), t3 = csub(a[1], a[4]), t4 = csub(a[2], a[3]);
  cf m1 = {a[0].x + c1 * t1.x + c2 * t2.x, a[0].y + c1 * t1.y + c2 * t2.y};
  cf m2 = {a[0].x + c2 * t1.x + c1 * t2.x, a[0].y + c2 * t1.y + c1 * t2.y};
  cf q1 = cmuli<DIR>(cf{s1 * t3.x + s2 * t4.x, s1 * t3.y + s2 * t4.y});
  cf q2 = cmuli<DIR>(cf{s2 * t3.x - s1 * t4.x, s2 * t3.y - s1 * t4.y});
  a[0] = {a[0].x + t1.x + t2.x, a[0].y + t1.y + t2.y};
  a[1] = cadd(m1, q1); a[4] = csub(m1, q1);
  a[2] = cadd(m2, q2); a[3] = csub(m2, q2);
}

template <int CTRL> __device__ __forceinline__ float dpp_add(float v) {
  int t = __builtin_amdgcn_update_dpp(0, __float_as_int(v), CTRL, 0xf, 0xf, true);
  return v + __int_as_float(t);
}
__device__ __forceinline__ float wave_sum(float v) {
  v = dpp_add<0x111>(v);
  v = dpp_add<0x112>(v);
  v = dpp_add<0x114>(v);
  v = dpp_add<0x118>(v);
  const float r0 = __int_as_float(__builtin_amdgcn_readlane(__float_as_int(v), 15));
  const float r1 = __int_as_float(__builtin_amdgcn_readlane(__float_as_int(v), 31));
  const float r2 = __int_as_float(__builtin_amdgcn_readlane(__float_as_int(v), 47));
  const float r3 = __int_as_float(__builtin_amdgcn_readlane(__float_as_int(v), 63));
  return (r0 + r1) + (r2 + r3);
}

__device__ __forceinline__ void layernorm10(float (&v)[10]) {
  float s = 0.f;
#pragma unroll
  for (int i = 0; i < 10; ++i) s += v[i];
  const float mean = wave_sum(s) * (1.0f / D_);
  float q = 0.f;
#pragma unroll
  for (int i = 0; i < 10; ++i) { float d = v[i] - mean; q += d * d; }
  const float var = wave_sum(q) * (1.0f / D_);
  const float m = bfr(mean);
  const float den = bfr(sqrtf(bfr(bfr(var) + 1e-5f)));
  const float rden = 1.0f / den;
#pragma unroll
  for (int i = 0; i < 10; ++i) {
    const float d = bfr(v[i] - m);
    float qq = d * rden;
    const float rem = __builtin_fmaf(-qq, den, d);
    qq = __builtin_fmaf(rem, rden, qq);
    v[i] = bfr(qq);
  }
}

__device__ __forceinline__ void prep_Wp(int idx, const u16* wr, const u16* wi, uint4* Wp) {
  if (idx >= DF_ * 256) return;
  const int k = idx % DF_, t = idx / DF_;
  const int p = t >> 6, r = (t >> 3) & 7, u = t & 7;
  const int base = p + 4 * r + 32 * u;
  unsigned o[8];
#pragma unroll
  for (int v = 0; v < 8; ++v) {
    const int ks = base + 256 * v;
    o[v] = (unsigned)wr[ks * DF_ + k] | ((unsigned)wi[ks * DF_ + k] << 16);
  }
  uint4* dst = Wp + (size_t)(k * 256 + t) * 2;
  dst[0] = make_uint4(o[0], o[1], o[2], o[3]);
  dst[1] = make_uint4(o[4], o[5], o[6], o[7]);
}
__device__ __forceinline__ void prep_frag(int idx, const u16* src, uint4* dst, int K, int N) {
  const int KT = K >> 5;
  if (idx >= (N >> 4) * KT * 64) return;
  const int lane = idx & 63, kt = (idx >> 6) % KT, nt = (idx >> 6) / KT;
  const int n = nt * 16 + (lane & 15), k0 = kt * 32 + (lane >> 4) * 8;
  unsigned o[4];
#pragma unroll
  for (int j = 0; j < 4; ++j)
    o[j] = (unsigned)src[(size_t)(k0 + 2 * j) * N + n] | ((unsigned)src[(size_t)(k0 + 2 * j + 1) * N + n] << 16);
  dst[idx] = make_uint4(o[0], o[1], o[2], o[3]);
}


#define PIN2(c) asm volatile("" : "+v"((c).x), "+v"((c).y))
__device__ __forceinline__ cf cmulf(cf a, cf b) { return {a.x * b.x - a.y * b.y, a.x * b.y + a.y * b.x}; }
__device__ __forceinline__ void pow7(cf w, cf (&t)[7]) {
  t[0] = w; t[1] = cmulf(w, w); t[2] = cmulf(t[1], w); t[3] = cmulf(t[1], t[1]);
  t[4] = cmulf(t[3], w); t[5] = cmulf(t[2], t[2]); t[6] = cmulf(t[3], t[2]);
}

constexpr int RS = 388;
__global__ __launch_bounds__(512) void k_row_fwd(const u16* __restrict__ x, cf* __restrict__ T,
                                                 const u16* __restrict__ wr, const u16* __restrict__ wi,
                                                 uint4* __restrict__ Wp) {
  if (blockIdx.x >= S_ / 8) {
    if (Wp) prep_Wp((blockIdx.x - S_ / 8) * 512 + threadIdx.x, wr, wi, Wp);
    return;
  }
  __shared__ cf buf[8 * RS];
  const int t = threadIdx.x, wv = t >> 6, lane = t & 63;
  const int row0 = blockIdx.x * 8;
  const int pB = lane >> 3, aB = lane & 7;
  unsigned raw[5];
  const unsigned* px = reinterpret_cast<const unsigned*>(x + (size_t)(row0 + wv) * D_) + lane;
#pragma unroll
  for (int i = 0; i < 5; ++i) raw[i] = px[64 * i];
  cf wA = g_tw640.v[2 * lane];
  cf wB = g_tw640.v[10 * aB];
  cf wP[6];
#pragma unroll
  for (int i = 0; i < 6; ++i) { int k = (t >> 3) + 64 * i; wP[i] = g_tw640.v[k > 320 ? 0 : k]; }
  float v[10];
#pragma unroll
  for (int i = 0; i < 5; ++i) { v[2 * i] = bf2f((u16)(raw[i] & 0xffff)); v[2 * i + 1] = bf2f((u16)(raw[i] >> 16)); }
  PIN2(wA); PIN2(wB);
#pragma unroll
  for (int i = 0; i < 6; ++i) PIN2(wP[i]);
  layernorm10(v);
  cf* R = buf + wv * RS;
  {
    cf z[5];
#pragma unroll
    for (int i = 0; i < 5; ++i) z[i] = {v[2 * i], v[2 * i + 1]};
    fft5<-1>(z);
    const cf w2 = cmulf(wA, wA), w3 = cmulf(w2, wA), w4 = cmulf(w2, w2);
    R[lane] = z[0];
    R[72 + lane] = ctw<-1>(z[1], wA);
    R[144 + lane] = ctw<-1>(z[2], w2);
    R[216 + lane] = ctw<-1>(z[3], w3);
    R[288 + lane] = ctw<-1>(z[4], w4);
  }
  __builtin_amdgcn_fence(__ATOMIC_SEQ_CST, "wavefront");
  if (lane < 40) {
    cf u[8], tw[7];
#pragma unroll
    for (int b = 0; b < 8; ++b) u[b] = R[pB * 72 + aB + 8 * b];
    pow7(wB, tw);
    fft8<-1>(u);
    __builtin_amdgcn_fence(__ATOMIC_SEQ_CST, "wavefront");
#pragma unroll
    for (int r = 0; r < 8; ++r) R[(r * 5 + pB) * 9 + aB] = r ? ctw<-1>(u[r], tw[r - 1]) : u[r];
    __builtin_amdgcn_fence(__ATOMIC_SEQ_CST, "wavefront");
#pragma unroll
    for (int a = 0; a < 8; ++a) u[a] = R[lane * 9 + a];
    fft8<-1>(u);
    __builtin_amdgcn_fence(__ATOMIC_SEQ_CST, "wavefront");
#pragma unroll
    for (int tt = 0; tt < 8; ++tt) R[lane + 40 * tt] = u[tt];
  }
  __syncthreads();
  {
    const int row = t & 7;
    const cf* Z = buf + row * RS;
#pragma unroll
    for (int i = 0; i < 6; ++i) {
      const int k = (t >> 3) + 64 * i;
      if (k <= 320) {
        const cf zk = Z[k == 320 ? 0 : k], zn = Z[k == 0 ? 0 : 320 - k];
        const cf E = {0.5f * (zk.x + zn.x), 0.5f * (zk.y - zn.y)};
        const cf O = {0.5f * (zk.y + zn.y), 0.5f * (zn.x - zk.x)};
        const cf tO = ctw<-1>(O, wP[i]);
        T[(size_t)k * S_ + row0 + row] = cf{E.x + tO.x, E.y + tO.y};
      }
    }
  }
}

constexpr int PR = 576;
__global__ __launch_bounds__(256) void k_col(float4* __restrict__ T, const uint4* __restrict__ Wp,
                                             const u16* __restrict__ wsrc, uint4* __restrict__ wdst) {
  if (blockIdx.x >= DF_) {
    if (wdst) prep_frag((blockIdx.x - DF_) * 256 + threadIdx.x, wsrc, wdst, D_, F_);
    return;
  }
  __shared__ cf buf[4 * PR];
  const int t = threadIdx.x, wv = t >> 6, lane = t & 63;
  const int col = blockIdx.x;
  float4* Tc = T + (size_t)col * (S_ / 2);
  const int n0 = 2 * t;
  const int c3 = lane & 7, r3 = lane >> 3;
  float4 q[4];
#pragma unroll
  for (int i = 0; i < 4; ++i) q[i] = Tc[t + 256 * i];
  uint4 wq0 = Wp[(size_t)(col * 256 + t) * 2], wq1 = Wp[(size_t)(col * 256 + t) * 2 + 1];
  cf w1a = g_tw2048.v[n0], w1b = g_tw2048.v[n0 + 1], w2b = g_tw2048.v[4 * lane], w3b = g_tw2048.v[32 * c3];
  PIN2(w1a); PIN2(w1b); PIN2(w2b); PIN2(w3b);
  asm volatile("" : "+v"(wq0.x), "+v"(wq0.y), "+v"(wq0.z), "+v"(wq0.w));
  asm volatile("" : "+v"(wq1.x), "+v"(wq1.y), "+v"(wq1.z), "+v"(wq1.w));
  cf tw1a[3], tw1b[3], tw2[7], tw3[7];
  tw1a[0] = w1a; tw1a[1] = cmulf(w1a, w1a); tw1a[2] = cmulf(tw1a[1], w1a);
  tw1b[0] = w1b; tw1b[1] = cmulf(w1b, w1b); tw1b[2] = cmulf(tw1b[1], w1b);
  {
    cf z0[4], z1[4];
#pragma unroll
    for (int i = 0; i < 4; ++i) { z0[i] = {q[i].x, q[i].y}; z1[i] = {q[i].z, q[i].w}; }
    fft4<-1>(z0[0], z0[1], z0[2], z0[3]);
    fft4<-1>(z1[0], z1[1], z1[2], z1[3]);
#pragma unroll
    for (int p = 0; p < 4; ++p) {
      cf u0 = z0[p], u1 = z1[p];
      if (p) { u0 = ctw<-1>(u0, tw1a[p - 1]); u1 = ctw<-1>(u1, tw1b[p - 1]); }
      *reinterpret_cast<float4*>(buf + p * PR + n0) = make_float4(u0.x, u0.y, u1.x, u1.y);
    }
  }
  pow7(w2b, tw2);
  pow7(w3b, tw3);
  __syncthreads();
  cf* R = buf + wv * PR;
  cf v[8];
#pragma unroll
  for (int b = 0; b < 8; ++b) v[b] = R[lane + 64 * b];
  fft8<-1>(v);
  __builtin_amdgcn_fence(__ATOMIC_SEQ_CST, "wavefront");
#pragma unroll
  for (int r = 0; r < 8; ++r) R[r * 72 + lane] = r ? ctw<-1>(v[r], tw2[r - 1]) : v[r];
  __builtin_amdgcn_fence(__ATOMIC_SEQ_CST, "wavefront");
#pragma unroll
  for (int e = 0; e < 8; ++e) v[e] = R[r3 * 72 + c3 + 8 * e];
  fft8<-1>(v);
  __builtin_amdgcn_fence(__ATOMIC_SEQ_CST, "wavefront");
#pragma unroll
  for (int u = 0; u < 8; ++u) R[r3 * 72 + c3 * 9 + u] = u ? ctw<-1>(v[u], tw3[u - 1]) : v[u];
  __builtin_amdgcn_fence(__ATOMIC_SEQ_CST, "wavefront");
#pragma unroll
  for (int c = 0; c < 8; ++c) v[c] = R[r3 * 72 + c * 9 + c3];
  fft8<-1>(v);
  {
    const unsigned w[8] = {wq0.x, wq0.y, wq0.z, wq0.w, wq1.x, wq1.y, wq1.z, wq1.w};
    constexpr float sc = 1.0f / ((float)S_ * (float)D_);
#pragma unroll
    for (int i = 0; i < 8; ++i) {
      const float a = bf2f((u16)(w[i] & 0xffff)) * sc, b = bf2f((u16)(w[i] >> 16)) * sc;
      const cf qq = v[i];
      v[i] = {qq.x * a - qq.y * b, qq.x * b + qq.y * a};
    }
  }
  fft8<1>(v);
  __builtin_amdgcn_fence(__ATOMIC_SEQ_CST, "wavefront");
#pragma unroll
  for (int c = 0; c < 8; ++c) R[r3 * 72 + c * 9 + c3] = v[c];
  __builtin_amdgcn_fence(__ATOMIC_SEQ_CST, "wavefront");
#pragma unroll
  for (int u = 0; u < 8; ++u) { cf qq = R[r3 * 72 + c3 * 9 + u]; v[u] = u ? ctw<1>(qq, tw3[u - 1]) : qq; }
  fft8<1>(v);
  __builtin_amdgcn_fence(__ATOMIC_SEQ_CST, "wavefront");
#pragma unroll
  for (int e = 0; e < 8; ++e) R[r3 * 72 + c3 + 8 * e] = v[e];
  __builtin_amdgcn_fence(__ATOMIC_SEQ_CST, "wavefront");
#pragma unroll
  for (int r = 0; r < 8; ++r) { cf qq = R[r * 72 + lane]; v[r] = r ? ctw<1>(qq, tw2[r - 1]) : qq; }
  fft8<1>(v);
  __builtin_amdgcn_fence(__ATOMIC_SEQ_CST, "wavefront");
#pragma unroll
  for (int b = 0; b < 8; ++b) R[lane + 64 * b] = v[b];
  __syncthreads();
  {
    cf z0[4], z1[4];
#pragma unroll
    for (int p = 0; p < 4; ++p) {
      float4 qq = *reinterpret_cast<const float4*>(buf + p * PR + n0);
      cf u0 = {qq.x, qq.y}, u1 = {qq.z, qq.w};
      if (p) { u0 = ctw<1>(u0, tw1a[p - 1]); u1 = ctw<1>(u1, tw1b[p - 1]); }
      z0[p] = u0; z1[p] = u1;
    }
    fft4<1>(z0[0], z0[1], z0[2], z0[3]);
    fft4<1>(z1[0], z1[1], z1[2], z1[3]);
#pragma unroll
    for (int i = 0; i < 4; ++i) Tc[t + 256 * i] = make_float4(z0[i].x, z0[i].y, z1[i].x, z1[i].y);
  }
}

__global__ __launch_bounds__(512) void k_row_inv(const cf* __restrict__ T, const u16* __restrict__ x,
                                                 u16* __restrict__ x1, u16* __restrict__ uf,
                                                 const u16* __restrict__ wsrc, uint4* __restrict__ wdst) {
  if (blockIdx.x >= S_ / 8) {
    if (wdst) prep_frag((blockIdx.x - S_ / 8) * 512 + threadIdx.x, wsrc, wdst, F_, D_);
    return;
  }
  __shared__ cf buf[8 * RS];
  const int t = threadIdx.x, wv = t >> 6, lane = t & 63;
  const int row0 = blockIdx.x * 8;
  const int pB = lane >> 3, aB = lane & 7;
  const int rC = lane / 5;
  cf xa[5], xb[5], wP[5];
  {
    const int row = t & 7;
#pragma unroll
    for (int i = 0; i < 5; ++i) {
      const int k = (t >> 3) + 64 * i;
      xa[i] = T[(size_t)k * S_ + row0 + row];
      xb[i] = T[(size_t)(320 - k) * S_ + row0 + row];
      wP[i] = g_tw640.v[k];
    }
  }
  unsigned raw[5];
  const unsigned* px = reinterpret_cast<const unsigned*>(x + (size_t)(row0 + wv) * D_) + lane;
#pragma unroll
  for (int i = 0; i < 5; ++i) raw[i] = px[64 * i];
  cf wA = g_tw640.v[2 * lane];
  cf wC = g_tw640.v[10 * (rC & 7)];
  PIN2(wA); PIN2(wC);
#pragma unroll
  for (int i = 0; i < 5; ++i) asm volatile("" : "+v"(raw[i]));
  {
    const int row = t & 7;
    cf* Z = buf + row * RS;
#pragma unroll
    for (int i = 0; i < 5; ++i) {
      const int k = (t >> 3) + 64 * i;
      cf a = xa[i], b = {xb[i].x, -xb[i].y};
      if (k == 0) { a.y = 0.f; b.y = 0.f; }
      const cf d = ctw<1>(csub(a, b), wP[i]);
      Z[k] = cf{a.x + b.x - d.y, a.y + b.y + d.x};
    }
  }
  __syncthreads();
  cf* R = buf + wv * RS;
  if (lane < 40) {
    cf u[8], tw[7];
#pragma unroll
    for (int tt = 0; tt < 8; ++tt) u[tt] = R[lane + 40 * tt];
    pow7(wC, tw);
    fft8<1>(u);
    __builtin_amdgcn_fence(__ATOMIC_SEQ_CST, "wavefront");
#pragma unroll
    for (int a = 0; a < 8; ++a) R[lane * 9 + a] = a ? ctw<1>(u[a], tw[a - 1]) : u[a];
    __builtin_amdgcn_fence(__ATOMIC_SEQ_CST, "wavefront");
#pragma unroll
    for (int r = 0; r < 8; ++r) u[r] = R[(r * 5 + pB) * 9 + aB];
    fft8<1>(u);
    __builtin_amdgcn_fence(__ATOMIC_SEQ_CST, "wavefront");
#pragma unroll
    for (int b = 0; b < 8; ++b) R[pB * 72 + aB + 8 * b] = u[b];
  }
  __builtin_amdgcn_fence(__ATOMIC_SEQ_CST, "wavefront");
  {
    cf z[5];
    const cf w2 = cmulf(wA, wA), w3 = cmulf(w2, wA), w4 = cmulf(w2, w2);
    z[0] = R[lane];
    z[1] = ctw<1>(R[72 + lane], wA);
    z[2] = ctw<1>(R[144 + lane], w2);
    z[3] = ctw<1>(R[216 + lane], w3);
    z[4] = ctw<1>(R[288 + lane], w4);
    fft5<1>(z);
    float v[10];
#pragma unroll
    for (int i = 0; i < 5; ++i) {
      v[2 * i] = bfr(bf2f((u16)(raw[i] & 0xffff)) + bfr(z[i].x));
      v[2 * i + 1] = bfr(bf2f((u16)(raw[i] >> 16)) + bfr(z[i].y));
    }
    const int row = row0 + wv;
    unsigned* o1 = reinterpret_cast<unsigned*>(x1 + (size_t)row * D_) + lane;
#pragma unroll
    for (int i = 0; i < 5; ++i) o1[64 * i] = pack2(v[2 * i], v[2 * i + 1]);
    layernorm10(v);
#pragma unroll
    for (int i = 0; i < 5; ++i) {
      const int d = 128 * i + 2 * lane;
      const size_t off = ((size_t)((row >> 4) * (D_ / 32) + (d >> 5)) * 64 + ((d >> 3) & 3) * 16 + (row & 15)) * 8 + (d & 7);
      *reinterpret_cast<unsigned*>(uf + off) = pack2(v[2 * i], v[2 * i + 1]);
    }
  }
}

__device__ __forceinline__ float gelu_tanh(float x) {
  const float y2 = x * (2.302208198f + 0.1029432397f * x * x);
  const float e = __builtin_amdgcn_exp2f(y2);
  return x - x * __builtin_amdgcn_rcpf(e + 1.0f);
}

constexpr int G1_NS = 10;
#define WAITVM(n) asm volatile("s_waitcnt vmcnt(" #n ") lgkmcnt(0)" ::: "memory")
__global__ __launch_bounds__(512, 1) void k_gemm1(const uint4* __restrict__ w1f, const uint4* __restrict__ uf,
                                                  const u16* __restrict__ b1, u16* __restrict__ hf) {
  extern __shared__ __attribute__((aligned(16))) char smem[];
  constexpr int KT = D_ / 32;
  constexpr int NS = G1_NS;
  constexpr int NR = KT / 2;
  const int bid = blockIdx.x;
  const int xcd = bid & 7, loc = bid >> 3;
  const int mt = xcd * 2 + (loc & 1), ft = loc >> 1;
  const int wv = __builtin_amdgcn_readfirstlane(threadIdx.x >> 6), lane = threadIdx.x & 63;
  const int ta = min(ft * 8 + wv, F_ / 16 - 1);
  const uint4* ga = w1f + (size_t)ta * KT * 64 + lane;
  const uint4* gb = uf + (size_t)(mt * 8 + wv) * KT * 64 + lane;
  char* lbase = smem + wv * 1024 + lane * 16;
#define G1_ISSUE(kt_) do { char* l_ = lbase + ((kt_) % NS) * 16384;                                  \
    __builtin_amdgcn_global_load_lds((const void*)(ga + (kt_) * 64), (void*)(l_), 16, 0, 0);          \
    __builtin_amdgcn_global_load_lds((const void*)(gb + (kt_) * 64), (void*)(l_ + 8192), 16, 0, 0); } while (0)
#pragma unroll
  for (int s = 0; s < NS; ++s) G1_ISSUE(s);
  const int grp = wv >> 2, w4 = wv & 3;
  const int wf = w4 >> 1, wm = w4 & 1;
  const int f0 = ft * 128 + wf * 64, m0 = mt * 128 + wm * 64;
  f32x4 acc[4][4];
#pragma unroll
  for (int i = 0; i < 4; ++i)
#pragma unroll
    for (int j = 0; j < 4; ++j) acc[i][j] = f32x4{0.f, 0.f, 0.f, 0.f};
  const char* ra = smem + grp * 16384 + (wf * 4) * 1024 + lane * 16;
  const char* rb = smem + grp * 16384 + (8 + wm * 4) * 1024 + lane * 16;
  bf16x8 A[2][4], B[2][4];
  WAITVM(16);
  __builtin_amdgcn_s_barrier();
  __builtin_amdgcn_sched_barrier(0);
#pragma unroll
  for (int i = 0; i < 4; ++i) {
    A[0][i] = *reinterpret_cast<const bf16x8*>(ra + i * 1024);
    B[0][i] = *reinterpret_cast<const bf16x8*>(rb + i * 1024);
  }
#pragma unroll
  for (int r = 0; r < NR; ++r) {
    if (r + 1 < NR) {
      constexpr int dummy = 0; (void)dummy;
      const int lastR = (r + 3 > 4 ? r + 3 : 4) < NR - 1 ? (r + 3 > 4 ? r + 3 : 4) : NR - 1;
      const int nout = 4 * (lastR - (r + 1));
      switch (nout) {
        case 0: WAITVM(0); break;
        case 4: WAITVM(4); break;
        case 8: WAITVM(8); break;
        case 12: WAITVM(12); break;
        default: WAITVM(16); break;
      }
      __builtin_amdgcn_s_barrier();
      __builtin_amdgcn_sched_barrier(0);
      if (r >= 1 && r + 4 < NR) { G1_ISSUE(2 * (r + 4)); G1_ISSUE(2 * (r + 4) + 1); }
      const int so = ((2 * (r + 1)) % NS) * 16384;
#pragma unroll
      for (int i = 0; i < 4; ++i) {
        A[(r + 1) & 1][i] = *reinterpret_cast<const bf16x8*>(ra + so + i * 1024);
        B[(r + 1) & 1][i] = *reinterpret_cast<const bf16x8*>(rb + so + i * 1024);
      }
    }
#pragma unroll
    for (int i = 0; i < 4; ++i)
#pragma unroll
      for (int j = 0; j < 4; ++j)
        acc[i][j] = __builtin_amdgcn_mfma_f32_16x16x32_bf16(A[r & 1][i], B[r & 1][j], acc[i][j], 0, 0, 0);
    __builtin_amdgcn_sched_barrier(0);
  }
#undef G1_ISSUE
  __syncthreads();
  f32x4* ex = reinterpret_cast<f32x4*>(smem) + (size_t)wv * 8 * 64 + lane;
#pragma unroll
  for (int ii = 0; ii < 2; ++ii)
#pragma unroll
    for (int j = 0; j < 4; ++j) ex[(ii * 4 + j) * 64] = grp ? acc[ii][j] : acc[2 + ii][j];
  __syncthreads();
  if (f0 >= F_) return;
  const f32x4* ey = reinterpret_cast<const f32x4*>(smem) + (size_t)(wv ^ 4) * 8 * 64 + lane;
#pragma unroll
  for (int ii = 0; ii < 2; ++ii) {
    const int f = f0 + 16 * (grp * 2 + ii) + (lane >> 4) * 4;
    const uint2 bb = *reinterpret_cast<const uint2*>(b1 + f);
    const float bv[4] = {bf2f((u16)(bb.x & 0xffff)), bf2f((u16)(bb.x >> 16)), bf2f((u16)(bb.y & 0xffff)), bf2f((u16)(bb.y >> 16))};
#pragma unroll
    for (int j = 0; j < 4; ++j) {
      const int m = m0 + 16 * j + (lane & 15);
      const f32x4 sacc = (grp ? acc[2 + ii][j] : acc[ii][j]) + ey[(ii * 4 + j) * 64];
      float h[4];
#pragma unroll
      for (int r = 0; r < 4; ++r) h[r] = gelu_tanh(bfr(bfr(sacc[r]) + bv[r]));
      const size_t off = ((size_t)((m >> 4) * (F_ / 32) + (f >> 5)) * 64 + ((f >> 3) & 3) * 16 + (m & 15)) * 8 + (f & 7);
      *reinterpret_cast<uint2*>(hf + off) = make_uint2(pack2(h[0], h[1]), pack2(h[2], h[3]));
    }
  }
}

constexpr int G2_ST = 7;
__global__ __launch_bounds__(256, 1) void k_gemm2(const bf16x8* __restrict__ w2f, const bf16x8* __restrict__ hf,
                                                  const u16* __restrict__ b2, const u16* __restrict__ x1,
                                                  u16* __restrict__ x2) {
  constexpr int KT = F_ / 32;
  constexpr int ST = G2_ST;
  __shared__ f32x4 red[4][15][64];
  const int bid = blockIdx.x;
  const int xcd = bid & 7, loc = bid >> 3;
  const int mt = xcd * 4 + (loc & 3), nt = loc >> 2;
  const int wv = __builtin_amdgcn_readfirstlane(threadIdx.x >> 6), lane = threadIdx.x & 63;
  const int n0 = nt * 80, m0 = mt * 64;
  const int k0 = wv < 2 ? wv * 14 : 28 + (wv - 2) * 13;
  const int kn = wv < 2 ? 14 : 13;
  const bf16x8* pa = w2f + ((size_t)(n0 >> 4) * KT + k0) * 64 + lane;
  const bf16x8* pb = hf + ((size_t)(m0 >> 4) * KT + k0) * 64 + lane;
  f32x4 acc[5][4];
#pragma unroll
  for (int i = 0; i < 5; ++i)
#pragma unroll
    for (int j = 0; j < 4; ++j) acc[i][j] = f32x4{0.f, 0.f, 0.f, 0.f};
  bf16x8 A[ST][5], B[ST][4];
#pragma unroll
  for (int s = 0; s < ST - 1; ++s) {
#pragma unroll
    for (int i = 0; i < 5; ++i) A[s][i] = pa[(i * KT + s) * 64];
#pragma unroll
    for (int j = 0; j < 4; ++j) B[s][j] = pb[(j * KT + s) * 64];
  }
  __builtin_amdgcn_sched_barrier(0);
#pragma unroll
  for (int kt = 0; kt < 14; ++kt) {
    const int nx = kt + ST - 1;
    if (nx < 13 || (nx == 13 && kn == 14)) {
#pragma unroll
      for (int i = 0; i < 5; ++i) A[nx % ST][i] = pa[(i * KT + nx) * 64];
#pragma unroll
      for (int j = 0; j < 4; ++j) B[nx % ST][j] = pb[(j * KT + nx) * 64];
    }
    if (kt < 13 || kn == 14) {
#pragma unroll
      for (int i = 0; i < 5; ++i)
#pragma unroll
        for (int j = 0; j < 4; ++j)
          acc[i][j] = __builtin_amdgcn_mfma_f32_16x16x32_bf16(A[kt % ST][i], B[kt % ST][j], acc[i][j], 0, 0, 0);
    }
    __builtin_amdgcn_sched_barrier(0);
  }
#pragma unroll
  for (int j = 0; j < 4; ++j) {
    if (j != wv) {
      const int slot = j > wv ? j - 1 : j;
#pragma unroll
      for (int i = 0; i < 5; ++i) red[wv][slot * 5 + i][lane] = acc[i][j];
    }
  }
  __syncthreads();
  f32x4 own[5];
#pragma unroll
  for (int i = 0; i < 5; ++i) own[i] = wv == 0 ? acc[i][0] : wv == 1 ? acc[i][1] : wv == 2 ? acc[i][2] : acc[i][3];
#pragma unroll
  for (int w = 0; w < 4; ++w) {
    if (w != wv) {
      const int slot = wv > w ? wv - 1 : wv;
#pragma unroll
      for (int i = 0; i < 5; ++i) own[i] += red[w][slot * 5 + i][lane];
    }
  }
  const int m = m0 + 16 * wv + (lane & 15);
#pragma unroll
  for (int i = 0; i < 5; ++i) {
    const int n = n0 + 16 * i + (lane >> 4) * 4;
    const uint2 bb = *reinterpret_cast<const uint2*>(b2 + n);
    const float bv[4] = {bf2f((u16)(bb.x & 0xffff)), bf2f((u16)(bb.x >> 16)), bf2f((u16)(bb.y & 0xffff)), bf2f((u16)(bb.y >> 16))};
    const uint2 xr = *reinterpret_cast<const uint2*>(x1 + (size_t)m * D_ + n);
    const float xv[4] = {bf2f((u16)(xr.x & 0xffff)), bf2f((u16)(xr.x >> 16)), bf2f((u16)(xr.y & 0xffff)), bf2f((u16)(xr.y >> 16))};
    float o[4];
#pragma unroll
    for (int r = 0; r < 4; ++r) o[r] = xv[r] + bfr(bfr(own[i][r]) + bv[r]);
    *reinterpret_cast<uint2*>(x2 + (size_t)m * D_ + n) = make_uint2(pack2(o[0], o[1]), pack2(o[2], o[3]));
  }
}

extern "C" void kernel_launch(void* const* d_in, const int* in_sizes, int n_in,
                              void* d_out, int out_size, void* d_ws, size_t ws_size,
                              hipStream_t stream) {
  const u16* x0 = (const u16*)d_in[0];
  const u16* wr = (const u16*)d_in[1];
  const u16* wi = (const u16*)d_in[2];
  const u16* w1 = (const u16*)d_in[4];
  const u16* b1 = (const u16*)d_in[5];
  const u16* w2 = (const u16*)d_in[6];
  const u16* b2 = (const u16*)d_in[7];
  u16* out = (u16*)d_out;

  char* ws = (char*)d_ws;
  size_t off = 0;
  auto carve = [&](size_t bytes) { char* p = ws + off; off += (bytes + 255) & ~(size_t)255; return p; };
  float4* T = (float4*)carve((size_t)DF_ * S_ * 8);
  uint4* Wp = (uint4*)carve((size_t)DF_ * 256 * 32);
  uint4* w1f = (uint4*)carve((size_t)D_ * F_ * 2);
  uint4* w2f = (uint4*)carve((size_t)D_ * F_ * 2);
  u16* xa = (u16*)carve((size_t)S_ * D_ * 2);
  u16* xb = (u16*)carve((size_t)S_ * D_ * 2);
  u16* uf = (u16*)carve((size_t)S_ * D_ * 2);
  u16* hf = (u16*)carve((size_t)S_ * F_ * 2);

  const int nWp = (DF_ * 256 + 511) / 512;
  const int nW1 = (D_ * F_ / 8 + 255) / 256;
  const int nW2 = (D_ * F_ / 8 + 511) / 512;
  constexpr int g1lds = G1_NS * 16384;
  hipFuncSetAttribute((const void*)k_gemm1, hipFuncAttributeMaxDynamicSharedMemorySize, g1lds);

  for (int layer = 0; layer < 2; ++layer) {
    const u16* xin = layer ? xb : x0;
    u16* xo = layer ? out : xb;
    k_row_fwd<<<S_ / 8 + (layer ? 0 : nWp), 512, 0, stream>>>(xin, (cf*)T, wr, wi, layer ? nullptr : Wp);
    k_col<<<DF_ + (layer ? 0 : nW1), 256, 0, stream>>>(T, Wp, w1, layer ? nullptr : w1f);
    k_row_inv<<<S_ / 8 + (layer ? 0 : nW2), 512, 0, stream>>>((const cf*)T, xin, xa, uf, w2, layer ? nullptr : w2f);
    k_gemm1<<<224, 512, g1lds, stream>>>(w1f, (const uint4*)uf, b1, hf);
    k_gemm2<<<256, 256, 0, stream>>>((const bf16x8*)w2f, (const bf16x8*)hf, b2, xa, xo);
  }
}
```

```cpp
#include <hip/hip_runtime.h>
#include <hip/hip_bf16.h>
#include <stdint.h>

typedef __attribute__((ext_vector_type(8))) short bf16x8;
typedef __attribute__((ext_vector_type(4))) float f32x4;
typedef unsigned short u16;

constexpr int S_ = 2048, D_ = 640, DF_ = 321, F_ = 1728;
#define KPAD 1
constexpr int KTD = D_ / 32 + KPAD;
constexpr int KTF = F_ / 32 + KPAD;
#define WARM1_BLOCKS 0

struct alignas(8) cf { float x, y; };
constexpr double kPi = 3.14159265358979323846264338327950288;
constexpr double c_sin(double x) {
  double term = x, sum = x;
  for (int i = 1; i < 26; ++i) { term *= -x * x / (double)((2 * i) * (2 * i + 1)); sum += term; }
  return sum;
}
constexpr double c_cos(double x) {
  double term = 1.0, sum = 1.0;
  for (int i = 1; i < 26; ++i) { term *= -x * x / (double)((2 * i - 1) * (2 * i)); sum += term; }
  return sum;
}
template <int N> struct TwTab {
  cf v[N];
  constexpr TwTab() : v{} {
    for (int i = 0; i < N; ++i) {
      double a = 2.0 * kPi * (double)i / (double)N;
      if (a > kPi) a -= 2.0 * kPi;
      v[i].x = (float)c_cos(a);
      v[i].y = (float)c_sin(a);
    }
  }
};
__device__ constexpr TwTab<2048> g_tw2048{};
__device__ constexpr TwTab<640> g_tw640{};

typedef unsigned int u32x2 __attribute__((ext_vector_type(2)));
__device__ __forceinline__ u32x2 ld8_early(const void* p) {
  const unsigned long long v = __hip_atomic_load(reinterpret_cast<const unsigned long long*>(p), __ATOMIC_RELAXED, __HIP_MEMORY_SCOPE_WAVEFRONT);
  return u32x2{(unsigned)v, (unsigned)(v >> 32)};
}
#define EARLY_WAIT(v) do {} while (0)
__device__ __forceinline__ float bf2f(u16 u) { return __uint_as_float(((unsigned)u) << 16); }
__device__ __forceinline__ u16 f2bf(float x) {
  __hip_bfloat16 h = __float2bfloat16(x);
  return *reinterpret_cast<u16*>(&h);
}
__device__ __forceinline__ float bfr(float x) { return bf2f(f2bf(x)); }
__device__ __forceinline__ unsigned pack2(float lo, float hi) {
  return (unsigned)f2bf(lo) | ((unsigned)f2bf(hi) << 16);
}

typedef float v2 __attribute__((ext_vector_type(2)));
__device__ __forceinline__ v2 swp(v2 a) { return __builtin_shufflevector(a, a, 1, 0); }
__device__ __forceinline__ v2 vfma(v2 a, v2 b, v2 c) { return __builtin_elementwise_fma(a, b, c); }
__device__ __forceinline__ v2 tw640(int i) { return *reinterpret_cast<const v2*>(&g_tw640.v[i]); }
__device__ __forceinline__ v2 tw2048(int i) { return *reinterpret_cast<const v2*>(&g_tw2048.v[i]); }
template <int DIR> __device__ __forceinline__ v2 addi(v2 a, v2 b) { return vfma(swp(b), DIR > 0 ? v2{-1.f, 1.f} : v2{1.f, -1.f}, a); }
template <int DIR> __device__ __forceinline__ v2 subi(v2 a, v2 b) { return vfma(swp(b), DIR > 0 ? v2{1.f, -1.f} : v2{-1.f, 1.f}, a); }
template <int DIR> __device__ __forceinline__ v2 ctw(v2 a, v2 w) {
  const v2 wx = __builtin_shufflevector(w, w, 0, 0);
  const v2 ws = DIR > 0 ? v2{-w.y, w.y} : v2{w.y, -w.y};
  return vfma(swp(a), ws, a * wx);
}
__device__ __forceinline__ v2 cmulf(v2 a, v2 b) { return ctw<1>(a, b); }
template <int DIR> __device__ __forceinline__ void fft4(v2& a0, v2& a1, v2& a2, v2& a3) {
  const v2 t0 = a0 + a2, t1 = a0 - a2, t2 = a1 + a3, t3 = a1 - a3;
  a0 = t0 + t2; a2 = t0 - t2; a1 = addi<DIR>(t1, t3); a3 = subi<DIR>(t1, t3);
}
template <int DIR> __device__ __forceinline__ void fft8(v2 (&a)[8]) {
  constexpr float R = 0.70710678118654752440f;
  v2 e0 = a[0], e1 = a[2], e2 = a[4], e3 = a[6], o0 = a[1], o1 = a[3], o2 = a[5], o3 = a[7];
  fft4<DIR>(e0, e1, e2, e3);
  fft4<DIR>(o0, o1, o2, o3);
  const v2 p1 = addi<DIR>(o1, o1), p3 = addi<DIR>(-o3, o3);
  const v2 RR = {R, R}, NR = {-R, -R};
  a[0] = e0 + o0; a[4] = e0 - o0;
  a[1] = vfma(p1, RR, e1); a[5] = vfma(p1, NR, e1);
  a[2] = addi<DIR>(e2, o2); a[6] = subi<DIR>(e2, o2);
  a[3] = vfma(p3, RR, e3); a[7] = vfma(p3, NR, e3);
}
template <int DIR> __device__ __forceinline__ void fft5(v2 (&a)[5]) {
  constexpr float c1 = 0.30901699437494742410f, c2 = -0.80901699437494742410f;
  constexpr float s1 = 0.95105651629515357212f, s2 = 0.58778525229247312917f;
  const v2 t1 = a[1] + a[4], t2 = a[2] + a[3], t3 = a[1] - a[4], t4 = a[2] - a[3];
  const v2 m1 = vfma(t2, v2{c2, c2}, vfma(t1, v2{c1, c1}, a[0]));
  const v2 m2 = vfma(t2, v2{c1, c1}, vfma(t1, v2{c2, c2}, a[0]));
  const v2 q1 = vfma(t4, v2{s2, s2}, t3 * v2{s1, s1});
  const v2 q2 = vfma(t4, v2{-s1, -s1}, t3 * v2{s2, s2});
  a[0] = a[0] + t1 + t2;
  a[1] = addi<DIR>(m1, q1); a[4] = subi<DIR>(m1, q1);
  a[2] = addi<DIR>(m2, q2); a[3] = subi<DIR>(m2, q2);
}

template <int CTRL> __device__ __forceinline__ float dpp_add(float v) {
  int t = __builtin_amdgcn_update_dpp(0, __float_as_int(v), CTRL, 0xf, 0xf, true);
  return v + __int_as_float(t);
}
__device__ __forceinline__ float wave_sum(float v) {
  v = dpp_add<0x111>(v);
  v = dpp_add<0x112>(v);
  v = dpp_add<0x114>(v);
  v = dpp_add<0x118>(v);
  const float r0 = __int_as_float(__builtin_amdgcn_readlane(__float_as_int(v), 15));
  const float r1 = __int_as_float(__builtin_amdgcn_readlane(__float_as_int(v), 31));
  const float r2 = __int_as_float(__builtin_amdgcn_readlane(__float_as_int(v), 47));
  const float r3 = __int_as_float(__builtin_amdgcn_readlane(__float_as_int(v), 63));
  return (r0 + r1) + (r2 + r3);
}

__device__ __forceinline__ void layernorm10(float (&v)[10]) {
  float s = 0.f;
#pragma unroll
  for (int i = 0; i < 10; ++i) s += v[i];
  const float mean = wave_sum(s) * (1.0f / D_);
  float q = 0.f;
#pragma unroll
  for (int i = 0; i < 10; ++i) { float d = v[i] - mean; q += d * d; }
  const float var = wave_sum(q) * (1.0f / D_);
  const float m = bfr(mean);
  const float den = bfr(sqrtf(bfr(bfr(var) + 1e-5f)));
  const float rden = 1.0f / den;
#pragma unroll
  for (int i = 0; i < 10; ++i) {
    const float d = bfr(v[i] - m);
    float qq = d * rden;
    const float rem = __builtin_fmaf(-qq, den, d);
    qq = __builtin_fmaf(rem, rden, qq);
    v[i] = bfr(qq);
  }
}

__device__ __forceinline__ void prep_Wp(int idx, const u16* wr, const u16* wi, uint4* Wp) {
  if (idx >= DF_ * 256) return;
  const int k = idx % DF_, t = idx / DF_;
  const int p = t >> 6, r = (t >> 3) & 7, u = t & 7;
  const int base = p + 4 * r + 32 * u;
  unsigned o[8];
#pragma unroll
  for (int v = 0; v < 8; ++v) {
    const int ks = base + 256 * v;
    o[v] = (unsigned)wr[ks * DF_ + k] | ((unsigned)wi[ks * DF_ + k] << 16);
  }
  uint4* dst = Wp + (size_t)(k * 256 + t) * 2;
  dst[0] = make_uint4(o[0], o[1], o[2], o[3]);
  dst[1] = make_uint4(o[4], o[5], o[6], o[7]);
}
__device__ __forceinline__ void prep_frag(int idx, const u16* src, uint4* dst, int K, int N) {
  const int KT = K >> 5;
  if (idx >= (N >> 4) * KT * 64) return;
  const int lane = idx & 63, kt = (idx >> 6) % KT, nt = (idx >> 6) / KT;
  const int n = nt * 16 + (lane & 15), k0 = kt * 32 + (lane >> 4) * 8;
  unsigned o[4];
#pragma unroll
  for (int j = 0; j < 4; ++j)
    o[j] = (unsigned)src[(size_t)(k0 + 2 * j) * N + n] | ((unsigned)src[(size_t)(k0 + 2 * j + 1) * N + n] << 16);
  dst[(size_t)(nt * (KT + KPAD) + kt) * 64 + lane] = make_uint4(o[0], o[1], o[2], o[3]);
}

#define PIN2(c) asm volatile("" : "+v"((c).x), "+v"((c).y))
__device__ __forceinline__ void pow7(v2 w, v2 (&t)[7]) {
  t[0] = w; t[1] = cmulf(w, w); t[2] = cmulf(t[1], w); t[3] = cmulf(t[1], t[1]);
  t[4] = cmulf(t[3], w); t[5] = cmulf(t[2], t[2]); t[6] = cmulf(t[3], t[2]);
}

__device__ __forceinline__ void l2_warm(int e, int nblk_per_xcd, const uint4* __restrict__ src, int n16, uint4* __restrict__ sink) {
  const int part = e >> 3;
  const int per = (n16 + nblk_per_xcd - 1) / nblk_per_xcd;
  const int beg = part * per, end = min(beg + per, n16);
  uint4 a = make_uint4(0, 0, 0, 0);
  for (int i = beg + threadIdx.x; i < end; i += blockDim.x * 4) {
    uint4 v0 = src[i], v1 = src[min(i + (int)blockDim.x, end - 1)], v2 = src[min(i + 2 * (int)blockDim.x, end - 1)], v3 = src[min(i + 3 * (int)blockDim.x, end - 1)];
    a.x ^= v0.x ^ v1.x ^ v2.x ^ v3.x; a.y ^= v0.y ^ v1.y ^ v2.y ^ v3.y;
  }
  if (a.x == 0x9e3779b9u && a.y == 0x7f4a7c15u && sink) sink[0] = a;
}

constexpr int RS = 388;
__global__ __launch_bounds__(512) void k_row_fwd(const u16* __restrict__ x, v2* __restrict__ T,
                                                 const u16* __restrict__ wr, const u16* __restrict__ wi,
                                                 uint4* __restrict__ Wp) {
  if (blockIdx.x >= S_ / 8) {
    if (Wp) prep_Wp((blockIdx.x - S_ / 8) * 512 + threadIdx.x, wr, wi, Wp);
    return;
  }
  __shared__ v2 buf[8 * RS];
  const int t = threadIdx.x, wv = t >> 6, lane = t & 63;
  const int row0 = ((blockIdx.x & 7) * 32 + (blockIdx.x >> 3)) * 8;
  const int pB = lane >> 3, aB = lane & 7;
  unsigned raw[5];
  const unsigned* px = reinterpret_cast<const unsigned*>(x + (size_t)(row0 + wv) * D_) + lane;
#pragma unroll
  for (int i = 0; i < 5; ++i) raw[i] = px[64 * i];
  v2 wA = tw640(2 * lane);
  v2 wB = tw640(10 * aB);
  v2 wP[6];
#pragma unroll
  for (int i = 0; i < 6; ++i) { int k = (t >> 3) + 64 * i; wP[i] = tw640(k > 320 ? 0 : k); }
  float v[10];
#pragma unroll
  for (int i = 0; i < 5; ++i) { v[2 * i] = bf2f((u16)(raw[i] & 0xffff)); v[2 * i + 1] = bf2f((u16)(raw[i] >> 16)); }
  PIN2(wA); PIN2(wB);
#pragma unroll
  for (int i = 0; i < 6; ++i) PIN2(wP[i]);
  layernorm10(v);
  v2* R = buf + wv * RS;
  {
    v2 z[5];
#pragma unroll
    for (int i = 0; i < 5; ++i) z[i] = {v[2 * i], v[2 * i + 1]};
    fft5<-1>(z);
    const v2 w2 = cmulf(wA, wA), w3 = cmulf(w2, wA), w4 = cmulf(w2, w2);
    R[lane] = z[0];
    R[72 + lane] = ctw<-1>(z[1], wA);
    R[144 + lane] = ctw<-1>(z[2], w2);
    R[216 + lane] = ctw<-1>(z[3], w3);
    R[288 + lane] = ctw<-1>(z[4], w4);
  }
  __builtin_amdgcn_fence(__ATOMIC_SEQ_CST, "wavefront");
  if (lane < 40) {
    v2 u[8], tw[7];
#pragma unroll
    for (int b = 0; b < 8; ++b) u[b] = R[pB * 72 + aB + 8 * b];
    pow7(wB, tw);
    fft8<-1>(u);
    __builtin_amdgcn_fence(__ATOMIC_SEQ_CST, "wavefront");
#pragma unroll
    for (int r = 0; r < 8; ++r) R[(r * 5 + pB) * 9 + aB] = r ? ctw<-1>(u[r], tw[r - 1]) : u[r];
    __builtin_amdgcn_fence(__ATOMIC_SEQ_CST, "wavefront");
#pragma unroll
    for (int a = 0; a < 8; ++a) u[a] = R[lane * 9 + a];
    fft8<-1>(u);
    __builtin_amdgcn_fence(__ATOMIC_SEQ_CST, "wavefront");
#pragma unroll
    for (int tt = 0; tt < 8; ++tt) R[lane + 40 * tt] = u[tt];
  }
  __syncthreads();
  {
    const int row = t & 7;
    const v2* Z = buf + row * RS;
#pragma unroll
    for (int i = 0; i < 6; ++i) {
      const int k = (t >> 3) + 64 * i;
      if (k <= 320) {
        const v2 zk = Z[k == 320 ? 0 : k], zn = Z[k == 0 ? 0 : 320 - k];
        const v2 E = {0.5f * (zk.x + zn.x), 0.5f * (zk.y - zn.y)};
        const v2 O = {0.5f * (zk.y + zn.y), 0.5f * (zn.x - zk.x)};
        const v2 tO = ctw<-1>(O, wP[i]);
        T[(size_t)k * S_ + row0 + row] = v2{E.x + tO.x, E.y + tO.y};
      }
    }
  }
}

constexpr int PR = 576;
__global__ __launch_bounds__(256) void k_col(float4* __restrict__ T, const uint4* __restrict__ Wp,
                                             const u16* __restrict__ wsrc, uint4* __restrict__ wdst) {
  if (blockIdx.x >= DF_) {
    if (wdst) prep_frag((blockIdx.x - DF_) * 256 + threadIdx.x, wsrc, wdst, D_, F_);
    return;
  }
  __shared__ v2 buf[4 * PR];
  const int t = threadIdx.x, wv = t >> 6, lane = t & 63;
  const int col = blockIdx.x;
  float4* Tc = T + (size_t)col * (S_ / 2);
  const int n0 = 2 * t;
  const int c3 = lane & 7, r3 = lane >> 3;
  float4 q[4];
#pragma unroll
  for (int i = 0; i < 4; ++i) q[i] = Tc[t + 256 * i];
  uint4 wq0 = Wp[(size_t)(col * 256 + t) * 2], wq1 = Wp[(size_t)(col * 256 + t) * 2 + 1];
  v2 w1a = tw2048(n0), w1b = tw2048(n0 + 1), w2b = tw2048(4 * lane), w3b = tw2048(32 * c3);
  PIN2(w1a); PIN2(w1b); PIN2(w2b); PIN2(w3b);
  asm volatile("" : "+v"(wq0.x), "+v"(wq0.y), "+v"(wq0.z), "+v"(wq0.w));
  asm volatile("" : "+v"(wq1.x), "+v"(wq1.y), "+v"(wq1.z), "+v"(wq1.w));
  v2 tw1a[3], tw1b[3], tw2[7], tw3[7];
  tw1a[0] = w1a; tw1a[1] = cmulf(w1a, w1a); tw1a[2] = cmulf(tw1a[1], w1a);
  tw1b[0] = w1b; tw1b[1] = cmulf(w1b, w1b); tw1b[2] = cmulf(tw1b[1], w1b);
  {
    v2 z0[4], z1[4];
#pragma unroll
    for (int i = 0; i < 4; ++i) { z0[i] = {q[i].x, q[i].y}; z1[i] = {q[i].z, q[i].w}; }
    fft4<-1>(z0[0], z0[1], z0[2], z0[3]);
    fft4<-1>(z1[0], z1[1], z1[2], z1[3]);
#pragma unroll
    for (int p = 0; p < 4; ++p) {
      v2 u0 = z0[p], u1 = z1[p];
      if (p) { u0 = ctw<-1>(u0, tw1a[p - 1]); u1 = ctw<-1>(u1, tw1b[p - 1]); }
      *reinterpret_cast<float4*>(buf + p * PR + n0) = make_float4(u0.x, u0.y, u1.x, u1.y);
    }
  }
  pow7(w2b, tw2);
  pow7(w3b, tw3);
  __syncthreads();
  v2* R = buf + wv * PR;
  v2 v[8];
#pragma unroll
  for (int b = 0; b < 8; ++b) v[b] = R[lane + 64 * b];
  fft8<-1>(v);
  __builtin_amdgcn_fence(__ATOMIC_SEQ_CST, "wavefront");
#pragma unroll
  for (int r = 0; r < 8; ++r) R[r * 72 + lane] = r ? ctw<-1>(v[r], tw2[r - 1]) : v[r];
  __builtin_amdgcn_fence(__ATOMIC_SEQ_CST, "wavefront");
#pragma unroll
  for (int e = 0; e < 8; ++e) v[e] = R[r3 * 72 + c3 + 8 * e];
  fft8<-1>(v);
  __builtin_amdgcn_fence(__ATOMIC_SEQ_CST, "wavefront");
#pragma unroll
  for (int u = 0; u < 8; ++u) R[r3 * 72 + c3 * 9 + u] = u ? ctw<-1>(v[u], tw3[u - 1]) : v[u];
  __builtin_amdgcn_fence(__ATOMIC_SEQ_CST, "wavefront");
#pragma unroll
  for (int c = 0; c < 8; ++c) v[c] = R[r3 * 72 + c * 9 + c3];
  fft8<-1>(v);
  {
    const unsigned w[8] = {wq0.x, wq0.y, wq0.z, wq0.w, wq1.x, wq1.y, wq1.z, wq1.w};
    constexpr float sc = 1.0f / ((float)S_ * (float)D_);
#pragma unroll
    for (int i = 0; i < 8; ++i) {
      const float a = bf2f((u16)(w[i] & 0xffff)) * sc, b = bf2f((u16)(w[i] >> 16)) * sc;
      const v2 qq = v[i];
      v[i] = {qq.x * a - qq.y * b, qq.x * b + qq.y * a};
    }
  }
  fft8<1>(v);
  __builtin_amdgcn_fence(__ATOMIC_SEQ_CST, "wavefront");
#pragma unroll
  for (int c = 0; c < 8; ++c) R[r3 * 72 + c * 9 + c3] = v[c];
  __builtin_amdgcn_fence(__ATOMIC_SEQ_CST, "wavefront");
#pragma unroll
  for (int u = 0; u < 8; ++u) { v2 qq = R[r3 * 72 + c3 * 9 + u]; v[u] = u ? ctw<1>(qq, tw3[u - 1]) : qq; }
  fft8<1>(v);
  __builtin_amdgcn_fence(__ATOMIC_SEQ_CST, "wavefront");
#pragma unroll
  for (int e = 0; e < 8; ++e) R[r3 * 72 + c3 + 8 * e] = v[e];
  __builtin_amdgcn_fence(__ATOMIC_SEQ_CST, "wavefront");
#pragma unroll
  for (int r = 0; r < 8; ++r) { v2 qq = R[r * 72 + lane]; v[r] = r ? ctw<1>(qq, tw2[r - 1]) : qq; }
  fft8<1>(v);
  __builtin_amdgcn_fence(__ATOMIC_SEQ_CST, "wavefront");
#pragma unroll
  for (int b = 0; b < 8; ++b) R[lane + 64 * b] = v[b];
  __syncthreads();
  {
    v2 z0[4], z1[4];
#pragma unroll
    for (int p = 0; p < 4; ++p) {
      float4 qq = *reinterpret_cast<const float4*>(buf + p * PR + n0);
      v2 u0 = {qq.x, qq.y}, u1 = {qq.z, qq.w};
      if (p) { u0 = ctw<1>(u0, tw1a[p - 1]); u1 = ctw<1>(u1, tw1b[p - 1]); }
      z0[p] = u0; z1[p] = u1;
    }
    fft4<1>(z0[0], z0[1], z0[2], z0[3]);
    fft4<1>(z1[0], z1[1], z1[2], z1[3]);
#pragma unroll
    for (int i = 0; i < 4; ++i) Tc[t + 256 * i] = make_float4(z0[i].x, z0[i].y, z1[i].x, z1[i].y);
  }
}

__global__ __launch_bounds__(512) void k_row_inv(const v2* __restrict__ T, const u16* __restrict__ x,
                                                 u16* __restrict__ x1, u16* __restrict__ uf,
                                                 const u16* __restrict__ wsrc, uint4* __restrict__ wdst,
                                                 const uint4* __restrict__ warm, uint4* __restrict__ sink) {
  if (blockIdx.x >= S_ / 8) {
    const int e = blockIdx.x - S_ / 8;
    if (e < WARM1_BLOCKS) l2_warm(e, WARM1_BLOCKS / 8, warm, D_ * F_ / 8, sink);
    else if (wdst) prep_frag((e - WARM1_BLOCKS) * 512 + threadIdx.x, wsrc, wdst, F_, D_);
    return;
  }
  __shared__ v2 buf[8 * RS];
  const int t = threadIdx.x, wv = t >> 6, lane = t & 63;
  const int row0 = ((blockIdx.x & 7) * 32 + (blockIdx.x >> 3)) * 8;
  const int pB = lane >> 3, aB = lane & 7;
  const int rC = lane / 5;
  v2 xa[5], xb[5], wP[5];
  {
    const int row = t & 7;
#pragma unroll
    for (int i = 0; i < 5; ++i) {
      const int k = (t >> 3) + 64 * i;
      xa[i] = T[(size_t)k * S_ + row0 + row];
      xb[i] = T[(size_t)(320 - k) * S_ + row0 + row];
      wP[i] = tw640(k);
    }
  }
  unsigned raw[5];
  const unsigned* px = reinterpret_cast<const unsigned*>(x + (size_t)(row0 + wv) * D_) + lane;
#pragma unroll
  for (int i = 0; i < 5; ++i) raw[i] = px[64 * i];
  v2 wA = tw640(2 * lane);
  v2 wC = tw640(10 * (rC & 7));
  PIN2(wA); PIN2(wC);
#pragma unroll
  for (int i = 0; i < 5; ++i) asm volatile("" : "+v"(raw[i]));
  {
    const int row = t & 7;
    v2* Z = buf + row * RS;
#pragma unroll
    for (int i = 0; i < 5; ++i) {
      const int k = (t >> 3) + 64 * i;
      v2 a = xa[i], b = {xb[i].x, -xb[i].y};
      if (k == 0) { a.y = 0.f; b.y = 0.f; }
      const v2 d = ctw<1>((a - b), wP[i]);
      Z[k] = v2{a.x + b.x - d.y, a.y + b.y + d.x};
    }
  }
  __syncthreads();
  v2* R = buf + wv * RS;
  if (lane < 40) {
    v2 u[8], tw[7];
#pragma unroll
    for (int tt = 0; tt < 8; ++tt) u[tt] = R[lane + 40 * tt];
    pow7(wC, tw);
    fft8<1>(u);
    __builtin_amdgcn_fence(__ATOMIC_SEQ_CST, "wavefront");
#pragma unroll
    for (int a = 0; a < 8; ++a) R[lane * 9 + a] = a ? ctw<1>(u[a], tw[a - 1]) : u[a];
    __builtin_amdgcn_fence(__ATOMIC_SEQ_CST, "wavefront");
#pragma unroll
    for (int r = 0; r < 8; ++r) u[r] = R[(r * 5 + pB) * 9 + aB];
    fft8<1>(u);
    __builtin_amdgcn_fence(__ATOMIC_SEQ_CST, "wavefront");
#pragma unroll
    for (int b = 0; b < 8; ++b) R[pB * 72 + aB + 8 * b] = u[b];
  }
  __builtin_amdgcn_fence(__ATOMIC_SEQ_CST, "wavefront");
  {
    v2 z[5];
    const v2 w2 = cmulf(wA, wA), w3 = cmulf(w2, wA), w4 = cmulf(w2, w2);
    z[0] = R[lane];
    z[1] = ctw<1>(R[72 + lane], wA);
    z[2] = ctw<1>(R[144 + lane], w2);
    z[3] = ctw<1>(R[216 + lane], w3);
    z[4] = ctw<1>(R[288 + lane], w4);
    fft5<1>(z);
    float v[10];
#pragma unroll
    for (int i = 0; i < 5; ++i) {
      v[2 * i] = bfr(bf2f((u16)(raw[i] & 0xffff)) + bfr(z[i].x));
      v[2 * i + 1] = bfr(bf2f((u16)(raw[i] >> 16)) + bfr(z[i].y));
    }
    const int row = row0 + wv;
    unsigned* o1 = reinterpret_cast<unsigned*>(x1 + (size_t)row * D_) + lane;
#pragma unroll
    for (int i = 0; i < 5; ++i) o1[64 * i] = pack2(v[2 * i], v[2 * i + 1]);
    layernorm10(v);
#pragma unroll
    for (int i = 0; i < 5; ++i) {
      const int d = 128 * i + 2 * lane;
      const size_t off = ((size_t)((row >> 4) * KTD + (d >> 5)) * 64 + ((d >> 3) & 3) * 16 + (row & 15)) * 8 + (d & 7);
      *reinterpret_cast<unsigned*>(uf + off) = pack2(v[2 * i], v[2 * i + 1]);
    }
  }
}

__device__ __forceinline__ float gelu_tanh(float x) {
  const float y2 = x * (2.302208198f + 0.1029432397f * x * x);
  const float e = __builtin_amdgcn_exp2f(y2);
  return x - x * __builtin_amdgcn_rcpf(e + 1.0f);
}

constexpr int G1_NS = 10;
#define WAITVM(n) asm volatile("s_waitcnt vmcnt(" #n ") lgkmcnt(0)" ::: "memory")
__global__ __launch_bounds__(512, 1) void k_gemm1(const uint4* __restrict__ w1f, const uint4* __restrict__ uf,
                                                  const u16* __restrict__ b1, u16* __restrict__ hf) {
  extern __shared__ __attribute__((aligned(16))) char smem[];
  constexpr int KT = D_ / 32;
  constexpr int NS = G1_NS;
  constexpr int NR = KT / 2;
  const int bid = blockIdx.x;
  const int xcd = bid & 7, loc = bid >> 3;
  const int mt = xcd * 2 + (loc & 1), ft = loc >> 1;
  const int wv = __builtin_amdgcn_readfirstlane(threadIdx.x >> 6), lane = threadIdx.x & 63;
  const int wf = wv >> 2, wm = wv & 3;
  const int f0 = ft * 128 + wf * 64, m0 = mt * 128 + wm * 32;
  u32x2 bb[4];
#pragma unroll
  for (int i = 0; i < 4; ++i) bb[i] = ld8_early(b1 + min(f0 + 16 * i + (lane >> 4) * 4, F_ - 4));
  const int ta = min(ft * 8 + wv, F_ / 16 - 1);
  const uint4* ga = w1f + (size_t)ta * KTD * 64 + lane;
  const uint4* gb = uf + (size_t)(mt * 8 + wv) * KTD * 64 + lane;
  char* lbase = smem + wv * 1024 + lane * 16;
#define G1_ISSUE(kt_) do { char* l_ = lbase + ((kt_) % NS) * 16384;                                  \
    __builtin_amdgcn_global_load_lds((const void*)(ga + (kt_) * 64), (void*)(l_), 16, 0, 0);          \
    __builtin_amdgcn_global_load_lds((const void*)(gb + (kt_) * 64), (void*)(l_ + 8192), 16, 0, 0); } while (0)
#pragma unroll
  for (int s = 0; s < NS; ++s) G1_ISSUE(s);
  f32x4 acc[4][2];
#pragma unroll
  for (int i = 0; i < 4; ++i) { acc[i][0] = f32x4{0.f, 0.f, 0.f, 0.f}; acc[i][1] = f32x4{0.f, 0.f, 0.f, 0.f}; }
  const char* ra = smem + (wf * 4) * 1024 + lane * 16;
  const char* rb = smem + (8 + wm * 2) * 1024 + lane * 16;
  bf16x8 A[2][2][4], B[2][2][2];
  WAITVM(16);
  __builtin_amdgcn_s_barrier();
  __builtin_amdgcn_sched_barrier(0);
#pragma unroll
  for (int h = 0; h < 2; ++h) {
#pragma unroll
    for (int i = 0; i < 4; ++i) A[0][h][i] = *reinterpret_cast<const bf16x8*>(ra + h * 16384 + i * 1024);
    B[0][h][0] = *reinterpret_cast<const bf16x8*>(rb + h * 16384);
    B[0][h][1] = *reinterpret_cast<const bf16x8*>(rb + h * 16384 + 1024);
  }
#pragma unroll
  for (int r = 0; r < NR; ++r) {
    if (r + 1 < NR) {
      const int lastR = (r + 3 > 4 ? r + 3 : 4) < NR - 1 ? (r + 3 > 4 ? r + 3 : 4) : NR - 1;
      const int nout = 4 * (lastR - (r + 1));
      switch (nout) {
        case 0: WAITVM(0); break;
        case 4: WAITVM(4); break;
        case 8: WAITVM(8); break;
        case 12: WAITVM(12); break;
        default: WAITVM(16); break;
      }
      __builtin_amdgcn_s_barrier();
      __builtin_amdgcn_sched_barrier(0);
      if (r >= 1 && r + 4 < NR) { G1_ISSUE(2 * (r + 4)); G1_ISSUE(2 * (r + 4) + 1); }
      const int so = ((2 * (r + 1)) % NS) * 16384;
#pragma unroll
      for (int h = 0; h < 2; ++h) {
#pragma unroll
        for (int i = 0; i < 4; ++i) A[(r + 1) & 1][h][i] = *reinterpret_cast<const bf16x8*>(ra + so + h * 16384 + i * 1024);
        B[(r + 1) & 1][h][0] = *reinterpret_cast<const bf16x8*>(rb + so + h * 16384);
        B[(r + 1) & 1][h][1] = *reinterpret_cast<const bf16x8*>(rb + so + h * 16384 + 1024);
      }
    }
#pragma unroll
    for (int h = 0; h < 2; ++h)
#pragma unroll
      for (int i = 0; i < 4; ++i)
#pragma unroll
        for (int j = 0; j < 2; ++j)
          acc[i][j] = __builtin_amdgcn_mfma_f32_16x16x32_bf16(A[r & 1][h][i], B[r & 1][h][j], acc[i][j], 0, 0, 0);
    __builtin_amdgcn_sched_barrier(0);
  }
#undef G1_ISSUE
  if (f0 >= F_) return;
#pragma unroll
  for (int i = 0; i < 4; ++i) EARLY_WAIT(bb[i]);
#pragma unroll
  for (int i = 0; i < 4; ++i) {
    const int f = f0 + 16 * i + (lane >> 4) * 4;
    const float bv[4] = {bf2f((u16)(bb[i].x & 0xffff)), bf2f((u16)(bb[i].x >> 16)), bf2f((u16)(bb[i].y & 0xffff)), bf2f((u16)(bb[i].y >> 16))};
#pragma unroll
    for (int j = 0; j < 2; ++j) {
      const int m = m0 + 16 * j + (lane & 15);
      float h[4];
#pragma unroll
      for (int r = 0; r < 4; ++r) h[r] = gelu_tanh(bfr(bfr(acc[i][j][r]) + bv[r]));
      const size_t off = ((size_t)((m >> 4) * KTF + (f >> 5)) * 64 + ((f >> 3) & 3) * 16 + (m & 15)) * 8 + (f & 7);
      *reinterpret_cast<uint2*>(hf + off) = make_uint2(pack2(h[0], h[1]), pack2(h[2], h[3]));
    }
  }
}

constexpr int G2_ST = 7;
__global__ __launch_bounds__(256, 1) void k_gemm2(const bf16x8* __restrict__ w2f, const bf16x8* __restrict__ hf,
                                                  const u16* __restrict__ b2, const u16* __restrict__ x1,
                                                  u16* __restrict__ x2) {
  constexpr int KT = F_ / 32;
  constexpr int ST = G2_ST;
  __shared__ f32x4 red[4][15][64];
  const int bid = blockIdx.x;
  const int xcd = bid & 7, loc = bid >> 3;
  const int mt = xcd * 4 + (loc & 3), nt = loc >> 2;
  const int wv = __builtin_amdgcn_readfirstlane(threadIdx.x >> 6), lane = threadIdx.x & 63;
  const int n0 = nt * 80, m0 = mt * 64;
  const int k0 = wv < 2 ? wv * 14 : 28 + (wv - 2) * 13;
  const int kn = wv < 2 ? 14 : 13;
  const bf16x8* pa = w2f + ((size_t)(n0 >> 4) * KTF + k0) * 64 + lane;
  const bf16x8* pb = hf + ((size_t)(m0 >> 4) * KTF + k0) * 64 + lane;
  u32x2 bb[5], xr[5];
  {
    const int m = m0 + 16 * wv + (lane & 15);
#pragma unroll
    for (int i = 0; i < 5; ++i) {
      const int n = n0 + 16 * i + (lane >> 4) * 4;
      bb[i] = ld8_early(b2 + n);
      xr[i] = ld8_early(x1 + (size_t)m * D_ + n);
    }
  }
  f32x4 acc[5][4];
#pragma unroll
  for (int i = 0; i < 5; ++i)
#pragma unroll
    for (int j = 0; j < 4; ++j) acc[i][j] = f32x4{0.f, 0.f, 0.f, 0.f};
  bf16x8 A[ST][5], B[ST][4];
#pragma unroll
  for (int s = 0; s < ST - 1; ++s) {
#pragma unroll
    for (int i = 0; i < 5; ++i) A[s][i] = pa[(i * KTF + s) * 64];
#pragma unroll
    for (int j = 0; j < 4; ++j) B[s][j] = pb[(j * KTF + s) * 64];
  }
  __builtin_amdgcn_sched_barrier(0);
#pragma unroll
  for (int kt = 0; kt < 14; ++kt) {
    const int nx = kt + ST - 1;
    if (nx < 13 || (nx == 13 && kn == 14)) {
#pragma unroll
      for (int i = 0; i < 5; ++i) A[nx % ST][i] = pa[(i * KTF + nx) * 64];
#pragma unroll
      for (int j = 0; j < 4; ++j) B[nx % ST][j] = pb[(j * KTF + nx) * 64];
    }
    if (kt < 13 || kn == 14) {
#pragma unroll
      for (int i = 0; i < 5; ++i)
#pragma unroll
        for (int j = 0; j < 4; ++j)
          acc[i][j] = __builtin_amdgcn_mfma_f32_16x16x32_bf16(A[kt % ST][i], B[kt % ST][j], acc[i][j], 0, 0, 0);
    }
    __builtin_amdgcn_sched_barrier(0);
  }
#pragma unroll
  for (int j = 0; j < 4; ++j) {
    if (j != wv) {
      const int slot = j > wv ? j - 1 : j;
#pragma unroll
      for (int i = 0; i < 5; ++i) red[wv][slot * 5 + i][lane] = acc[i][j];
    }
  }
  __syncthreads();
  f32x4 own[5];
#pragma unroll
  for (int i = 0; i < 5; ++i) own[i] = wv == 0 ? acc[i][0] : wv == 1 ? acc[i][1] : wv == 2 ? acc[i][2] : acc[i][3];
#pragma unroll
  for (int w = 0; w < 4; ++w) {
    if (w != wv) {
      const int slot = wv > w ? wv - 1 : wv;
#pragma unroll
      for (int i = 0; i < 5; ++i) own[i] += red[w][slot * 5 + i][lane];
    }
  }
  const int m = m0 + 16 * wv + (lane & 15);
#pragma unroll
  for (int i = 0; i < 5; ++i) { EARLY_WAIT(bb[i]); EARLY_WAIT(xr[i]); }
#pragma unroll
  for (int i = 0; i < 5; ++i) {
    const int n = n0 + 16 * i + (lane >> 4) * 4;
    const float bv[4] = {bf2f((u16)(bb[i].x & 0xffff)), bf2f((u16)(bb[i].x >> 16)), bf2f((u16)(bb[i].y & 0xffff)), bf2f((u16)(bb[i].y >> 16))};
    const float xv[4] = {bf2f((u16)(xr[i].x & 0xffff)), bf2f((u16)(xr[i].x >> 16)), bf2f((u16)(xr[i].y & 0xffff)), bf2f((u16)(xr[i].y >> 16))};
    float o[4];
#pragma unroll
    for (int r = 0; r < 4; ++r) o[r] = xv[r] + bfr(bfr(own[i][r]) + bv[r]);
    *reinterpret_cast<uint2*>(x2 + (size_t)m * D_ + n) = make_uint2(pack2(o[0], o[1]), pack2(o[2], o[3]));
  }
}

extern "C" void kernel_launch(void* const* d_in, const int* in_sizes, int n_in,
                              void* d_out, int out_size, void* d_ws, size_t ws_size,
                              hipStream_t stream) {
  const u16* x0 = (const u16*)d_in[0];
  const u16* wr = (const u16*)d_in[1];
  const u16* wi = (const u16*)d_in[2];
  const u16* w1 = (const u16*)d_in[4];
  const u16* b1 = (const u16*)d_in[5];
  const u16* w2 = (const u16*)d_in[6];
  const u16* b2 = (const u16*)d_in[7];
  u16* out = (u16*)d_out;

  char* ws = (char*)d_ws;
  size_t off = 0;
  auto carve = [&](size_t bytes) { char* p = ws + off; off += (bytes + 255) & ~(size_t)255; return p; };
  float4* T = (float4*)carve((size_t)DF_ * S_ * 8);
  uint4* Wp = (uint4*)carve((size_t)DF_ * 256 * 32);
  uint4* w1f = (uint4*)carve((size_t)(F_ / 16) * KTD * 1024);
  uint4* w2f = (uint4*)carve((size_t)(D_ / 16) * KTF * 1024);
  u16* xa = (u16*)carve((size_t)S_ * D_ * 2);
  u16* xb = (u16*)carve((size_t)S_ * D_ * 2);
  u16* uf = (u16*)carve((size_t)(S_ / 16) * KTD * 1024);
  u16* hf = (u16*)carve((size_t)(S_ / 16) * KTF * 1024);
  uint4* sink = (uint4*)carve(256);

  const int nWp = (DF_ * 256 + 511) / 512;
  const int nW1 = (D_ * F_ / 8 + 255) / 256;
  const int nW2 = (D_ * F_ / 8 + 511) / 512;
  constexpr int g1lds = G1_NS * 16384;
  hipFuncSetAttribute((const void*)k_gemm1, hipFuncAttributeMaxDynamicSharedMemorySize, g1lds);

  for (int layer = 0; layer < 2; ++layer) {
    const u16* xin = layer ? xb : x0;
    u16* xo = layer ? out : xb;
    k_row_fwd<<<S_ / 8 + (layer ? 0 : nWp), 512, 0, stream>>>(xin, (v2*)T, wr, wi, layer ? nullptr : Wp);
    k_col<<<DF_ + (layer ? 0 : nW1), 256, 0, stream>>>(T, Wp, w1, layer ? nullptr : w1f);
    k_row_inv<<<S_ / 8 + WARM1_BLOCKS + (layer ? 0 : nW2), 512, 0, stream>>>((const v2*)T, xin, xa, uf, w2, layer ? nullptr : w2f, w1f, sink);
    k_gemm1<<<224, 512, g1lds, stream>>>(w1f, (const uint4*)uf, b1, hf);
    k_gemm2<<<256, 256, 0, stream>>>((const bf16x8*)w2f, (const bf16x8*)hf, b2, xa, xo);
  }
}
```

```cpp
#include <hip/hip_runtime.h>
#include <hip/hip_bf16.h>
#include <stdint.h>

typedef __attribute__((ext_vector_type(8))) short bf16x8;
typedef __attribute__((ext_vector_type(4))) float f32x4;
typedef unsigned short u16;

constexpr int S_ = 2048, D_ = 640, DF_ = 321, F_ = 1728;
#define KPAD 1
constexpr int KTD = D_ / 32 + KPAD;
constexpr int KTF = F_ / 32 + KPAD;

struct alignas(8) cf { float x, y; };
constexpr double kPi = 3.14159265358979323846264338327950288;
constexpr double c_sin(double x) {
  double term = x, sum = x;
  for (int i = 1; i < 26; ++i) { term *= -x * x / (double)((2 * i) * (2 * i + 1)); sum += term; }
  return sum;
}
constexpr double c_cos(double x) {
  double term = 1.0, sum = 1.0;
  for (int i = 1; i < 26; ++i) { term *= -x * x / (double)((2 * i - 1) * (2 * i)); sum += term; }
  return sum;
}
template <int N> struct TwTab {
  cf v[N];
  constexpr TwTab() : v{} {
    for (int i = 0; i < N; ++i) {
      double a = 2.0 * kPi * (double)i / (double)N;
      if (a > kPi) a -= 2.0 * kPi;
      v[i].x = (float)c_cos(a);
      v[i].y = (float)c_sin(a);
    }
  }
};
__device__ constexpr TwTab<2048> g_tw2048{};
__device__ constexpr TwTab<640> g_tw640{};

typedef unsigned int u32x2 __attribute__((ext_vector_type(2)));
__device__ __forceinline__ u32x2 ld8_early(const void* p) {
  const unsigned long long v = __hip_atomic_load(reinterpret_cast<const unsigned long long*>(p), __ATOMIC_RELAXED, __HIP_MEMORY_SCOPE_WAVEFRONT);
  return u32x2{(unsigned)v, (unsigned)(v >> 32)};
}
__device__ __forceinline__ float bf2f(u16 u) { return __uint_as_float(((unsigned)u) << 16); }
__device__ __forceinline__ u16 f2bf(float x) {
  __hip_bfloat16 h = __float2bfloat16(x);
  return *reinterpret_cast<u16*>(&h);
}
__device__ __forceinline__ float bfr(float x) { return bf2f(f2bf(x)); }
__device__ __forceinline__ unsigned pack2(float lo, float hi) {
  return (unsigned)f2bf(lo) | ((unsigned)f2bf(hi) << 16);
}

typedef float v2 __attribute__((ext_vector_type(2)));
__device__ __forceinline__ v2 swp(v2 a) { return __builtin_shufflevector(a, a, 1, 0); }
__device__ __forceinline__ v2 vfma(v2 a, v2 b, v2 c) { return __builtin_elementwise_fma(a, b, c); }
__device__ __forceinline__ v2 tw640(int i) { return *reinterpret_cast<const v2*>(&g_tw640.v[i]); }
__device__ __forceinline__ v2 tw2048(int i) { return *reinterpret_cast<const v2*>(&g_tw2048.v[i]); }
template <int DIR> __device__ __forceinline__ v2 addi(v2 a, v2 b) { return vfma(swp(b), DIR > 0 ? v2{-1.f, 1.f} : v2{1.f, -1.f}, a); }
template <int DIR> __device__ __forceinline__ v2 subi(v2 a, v2 b) { return vfma(swp(b), DIR > 0 ? v2{1.f, -1.f} : v2{-1.f, 1.f}, a); }
template <int DIR> __device__ __forceinline__ v2 ctw(v2 a, v2 w) {
  const v2 wx = __builtin_shufflevector(w, w, 0, 0);
  const v2 ws = DIR > 0 ? v2{-w.y, w.y} : v2{w.y, -w.y};
  return vfma(swp(a), ws, a * wx);
}
__device__ __forceinline__ v2 cmulf(v2 a, v2 b) { return ctw<1>(a, b); }
template <int DIR> __device__ __forceinline__ void fft4(v2& a0, v2& a1, v2& a2, v2& a3) {
  const v2 t0 = a0 + a2, t1 = a0 - a2, t2 = a1 + a3, t3 = a1 - a3;
  a0 = t0 + t2; a2 = t0 - t2; a1 = addi<DIR>(t1, t3); a3 = subi<DIR>(t1, t3);
}
template <int DIR> __device__ __forceinline__ void fft8(v2 (&a)[8]) {
  constexpr float R = 0.70710678118654752440f;
  v2 e0 = a[0], e1 = a[2], e2 = a[4], e3 = a[6], o0 = a[1], o1 = a[3], o2 = a[5], o3 = a[7];
  fft4<DIR>(e0, e1, e2, e3);
  fft4<DIR>(o0, o1, o2, o3);
  const v2 p1 = addi<DIR>(o1, o1), p3 = addi<DIR>(-o3, o3);
  const v2 RR = {R, R}, NR = {-R, -R};
  a[0] = e0 + o0; a[4] = e0 - o0;
  a[1] = vfma(p1, RR, e1); a[5] = vfma(p1, NR, e1);
  a[2] = addi<DIR>(e2, o2); a[6] = subi<DIR>(e2, o2);
  a[3] = vfma(p3, RR, e3); a[7] = vfma(p3, NR, e3);
}
template <int DIR> __device__ __forceinline__ void fft5(v2 (&a)[5]) {
  constexpr float c1 = 0.30901699437494742410f, c2 = -0.80901699437494742410f;
  constexpr float s1 = 0.95105651629515357212f, s2 = 0.58778525229247312917f;
  const v2 t1 = a[1] + a[4], t2 = a[2] + a[3], t3 = a[1] - a[4], t4 = a[2] - a[3];
  const v2 m1 = vfma(t2, v2{c2, c2}, vfma(t1, v2{c1, c1}, a[0]));
  const v2 m2 = vfma(t2, v2{c1, c1}, vfma(t1, v2{c2, c2}, a[0]));
  const v2 q1 = vfma(t4, v2{s2, s2}, t3 * v2{s1, s1});
  const v2 q2 = vfma(t4, v2{-s1, -s1}, t3 * v2{s2, s2});
  a[0] = a[0] + t1 + t2;
  a[1] = addi<DIR>(m1, q1); a[4] = subi<DIR>(m1, q1);
  a[2] = addi<DIR>(m2, q2); a[3] = subi<DIR>(m2, q2);
}

template <int CTRL> __device__ __forceinline__ float dpp_add(float v) {
  int t = __builtin_amdgcn_update_dpp(0, __float_as_int(v), CTRL, 0xf, 0xf, true);
  return v + __int_as_float(t);
}
__device__ __forceinline__ float wave_sum(float v) {
  v = dpp_add<0x111>(v);
  v = dpp_add<0x112>(v);
  v = dpp_add<0x114>(v);
  v = dpp_add<0x118>(v);
  const float r0 = __int_as_float(__builtin_amdgcn_readlane(__float_as_int(v), 15));
  const float r1 = __int_as_float(__builtin_amdgcn_readlane(__float_as_int(v), 31));
  const float r2 = __int_as_float(__builtin_amdgcn_readlane(__float_as_int(v), 47));
  const float r3 = __int_as_float(__builtin_amdgcn_readlane(__float_as_int(v), 63));
  return (r0 + r1) + (r2 + r3);
}

__device__ __forceinline__ void layernorm10(float (&v)[10]) {
  float s = 0.f;
#pragma unroll
  for (int i = 0; i < 10; ++i) s += v[i];
  const float mean = wave_sum(s) * (1.0f / D_);
  float q = 0.f;
#pragma unroll
  for (int i = 0; i < 10; ++i) { float d = v[i] - mean; q += d * d; }
  const float var = wave_sum(q) * (1.0f / D_);
  const float m = bfr(mean);
  const float den = bfr(sqrtf(bfr(bfr(var) + 1e-5f)));
  const float rden = 1.0f / den;
#pragma unroll
  for (int i = 0; i < 10; ++i) {
    const float d = bfr(v[i] - m);
    float qq = d * rden;
    const float rem = __builtin_fmaf(-qq, den, d);
    qq = __builtin_fmaf(rem, rden, qq);
    v[i] = bfr(qq);
  }
}

__device__ __forceinline__ void prep_Wp(int idx, const u16* wr, const u16* wi, uint4* Wp) {
  if (idx >= DF_ * 256) return;
  const int k = idx % DF_, t = idx / DF_;
  const int p = t >> 6, r = (t >> 3) & 7, u = t & 7;
  const int base = p + 4 * r + 32 * u;
  unsigned o[8];
#pragma unroll
  for (int v = 0; v < 8; ++v) {
    const int ks = base + 256 * v;
    o[v] = (unsigned)wr[ks * DF_ + k] | ((unsigned)wi[ks * DF_ + k] << 16);
  }
  uint4* dst = Wp + (size_t)(k * 256 + t) * 2;
  dst[0] = make_uint4(o[0], o[1], o[2], o[3]);
  dst[1] = make_uint4(o[4], o[5], o[6], o[7]);
}
__device__ __forceinline__ void prep_frag(int tile, int t, u16* lds, const u16* __restrict__ src, uint4* __restrict__ dst, int K, int N) {
  const int KT = K >> 5, NT = N >> 6;
  const bool ok = tile < KT * NT;
  const int kt = tile % KT, nb = tile / KT;
  if (ok) {
    const int kr = t >> 3, c = t & 7;
    const uint4 v = *reinterpret_cast<const uint4*>(src + (size_t)(kt * 32 + kr) * N + nb * 64 + c * 8);
    *reinterpret_cast<uint4*>(lds + kr * 72 + c * 8) = v;
  }
  __syncthreads();
  if (ok) {
    const int ntl = t >> 6, lane = t & 63;
    const int n = ntl * 16 + (lane & 15), k0 = (lane >> 4) * 8;
    unsigned o[4];
#pragma unroll
    for (int j = 0; j < 4; ++j) o[j] = (unsigned)lds[(k0 + 2 * j) * 72 + n] | ((unsigned)lds[(k0 + 2 * j + 1) * 72 + n] << 16);
    dst[(size_t)((nb * 4 + ntl) * (KT + KPAD) + kt) * 64 + lane] = make_uint4(o[0], o[1], o[2], o[3]);
  }
}

#define PIN2(c) asm volatile("" : "+v"((c).x), "+v"((c).y))
__device__ __forceinline__ void pow7(v2 w, v2 (&t)[7]) {
  t[0] = w; t[1] = cmulf(w, w); t[2] = cmulf(t[1], w); t[3] = cmulf(t[1], t[1]);
  t[4] = cmulf(t[3], w); t[5] = cmulf(t[2], t[2]); t[6] = cmulf(t[3], t[2]);
}

constexpr int RS = 388;
__global__ __launch_bounds__(512) void k_row_fwd(const u16* __restrict__ x, v2* __restrict__ T,
                                                 const u16* __restrict__ wr, const u16* __restrict__ wi,
                                                 uint4* __restrict__ Wp) {
  if (blockIdx.x >= S_ / 8) {
    if (Wp) prep_Wp((blockIdx.x - S_ / 8) * 512 + threadIdx.x, wr, wi, Wp);
    return;
  }
  __shared__ __attribute__((aligned(16))) v2 buf[8 * RS + 321 * 10];
  v2* XT = buf + 8 * RS;
  const int t = threadIdx.x, wv = t >> 6, lane = t & 63;
  const int row0 = ((blockIdx.x & 7) * 32 + (blockIdx.x >> 3)) * 8;
  const int pB = lane >> 3, aB = lane & 7;
  unsigned raw[5];
  const unsigned* px = reinterpret_cast<const unsigned*>(x + (size_t)(row0 + wv) * D_) + lane;
#pragma unroll
  for (int i = 0; i < 5; ++i) raw[i] = px[64 * i];
  v2 wA = tw640(2 * lane);
  v2 wB = tw640(10 * aB);
  v2 wP[6];
#pragma unroll
  for (int i = 0; i < 6; ++i) { int k = lane + 64 * i; wP[i] = tw640(k > 320 ? 0 : k); }
  float v[10];
#pragma unroll
  for (int i = 0; i < 5; ++i) { v[2 * i] = bf2f((u16)(raw[i] & 0xffff)); v[2 * i + 1] = bf2f((u16)(raw[i] >> 16)); }
  PIN2(wA); PIN2(wB);
#pragma unroll
  for (int i = 0; i < 6; ++i) PIN2(wP[i]);
  layernorm10(v);
  v2* R = buf + wv * RS;
  {
    v2 z[5];
#pragma unroll
    for (int i = 0; i < 5; ++i) z[i] = v2{v[2 * i], v[2 * i + 1]};
    fft5<-1>(z);
    const v2 w2 = cmulf(wA, wA), w3 = cmulf(w2, wA), w4 = cmulf(w2, w2);
    R[lane] = z[0];
    R[72 + lane] = ctw<-1>(z[1], wA);
    R[144 + lane] = ctw<-1>(z[2], w2);
    R[216 + lane] = ctw<-1>(z[3], w3);
    R[288 + lane] = ctw<-1>(z[4], w4);
  }
  __builtin_amdgcn_fence(__ATOMIC_SEQ_CST, "wavefront");
  if (lane < 40) {
    v2 u[8], tw[7];
#pragma unroll
    for (int b = 0; b < 8; ++b) u[b] = R[pB * 72 + aB + 8 * b];
    pow7(wB, tw);
    fft8<-1>(u);
    __builtin_amdgcn_fence(__ATOMIC_SEQ_CST, "wavefront");
#pragma unroll
    for (int r = 0; r < 8; ++r) R[(r * 5 + pB) * 9 + aB] = r ? ctw<-1>(u[r], tw[r - 1]) : u[r];
    __builtin_amdgcn_fence(__ATOMIC_SEQ_CST, "wavefront");
#pragma unroll
    for (int a = 0; a < 8; ++a) u[a] = R[lane * 9 + a];
    fft8<-1>(u);
    __builtin_amdgcn_fence(__ATOMIC_SEQ_CST, "wavefront");
#pragma unroll
    for (int tt = 0; tt < 8; ++tt) R[lane + 40 * tt] = u[tt];
  }
  __builtin_amdgcn_fence(__ATOMIC_SEQ_CST, "wavefront");
#pragma unroll
  for (int i = 0; i < 6; ++i) {
    const int k = lane + 64 * i;
    if (k <= 320) {
      const v2 zk = R[k == 320 ? 0 : k], zn = R[k == 0 ? 0 : 320 - k];
      const v2 E = v2{0.5f * (zk.x + zn.x), 0.5f * (zk.y - zn.y)};
      const v2 O = v2{0.5f * (zk.y + zn.y), 0.5f * (zn.x - zk.x)};
      XT[k * 10 + wv] = E + ctw<-1>(O, wP[i]);
    }
  }
  __syncthreads();
#pragma unroll
  for (int i = 0; i < 3; ++i) {
    const int idx = t + 512 * i;
    if (idx < DF_ * 4) {
      const int k = idx >> 2, q = idx & 3;
      const f32x4 o = *reinterpret_cast<const f32x4*>(XT + k * 10 + 2 * q);
      *reinterpret_cast<f32x4*>(T + (size_t)k * S_ + row0 + 2 * q) = o;
    }
  }
}

constexpr int PR = 576;
__global__ __launch_bounds__(256) void k_col(float4* __restrict__ T, const uint4* __restrict__ Wp,
                                             const u16* __restrict__ wsrc, uint4* __restrict__ wdst) {
  __shared__ v2 buf[4 * PR];
  if (blockIdx.x >= DF_) {
    prep_frag(blockIdx.x - DF_, threadIdx.x, reinterpret_cast<u16*>(buf), wsrc, wdst, D_, F_);
    return;
  }
  const int t = threadIdx.x, wv = t >> 6, lane = t & 63;
  const int col = blockIdx.x;
  float4* Tc = T + (size_t)col * (S_ / 2);
  const int n0 = 2 * t;
  const int c3 = lane & 7, r3 = lane >> 3;
  float4 q[4];
#pragma unroll
  for (int i = 0; i < 4; ++i) q[i] = Tc[t + 256 * i];
  uint4 wq0 = Wp[(size_t)(col * 256 + t) * 2], wq1 = Wp[(size_t)(col * 256 + t) * 2 + 1];
  v2 w1a = tw2048(n0), w1b = tw2048(n0 + 1), w2b = tw2048(4 * lane), w3b = tw2048(32 * c3);
  PIN2(w1a); PIN2(w1b); PIN2(w2b); PIN2(w3b);
  asm volatile("" : "+v"(wq0.x), "+v"(wq0.y), "+v"(wq0.z), "+v"(wq0.w));
  asm volatile("" : "+v"(wq1.x), "+v"(wq1.y), "+v"(wq1.z), "+v"(wq1.w));
  v2 tw1a[3], tw1b[3], tw2[7], tw3[7];
  tw1a[0] = w1a; tw1a[1] = cmulf(w1a, w1a); tw1a[2] = cmulf(tw1a[1], w1a);
  tw1b[0] = w1b; tw1b[1] = cmulf(w1b, w1b); tw1b[2] = cmulf(tw1b[1], w1b);
  {
    v2 z0[4], z1[4];
#pragma unroll
    for (int i = 0; i < 4; ++i) { z0[i] = {q[i].x, q[i].y}; z1[i] = {q[i].z, q[i].w}; }
    fft4<-1>(z0[0], z0[1], z0[2], z0[3]);
    fft4<-1>(z1[0], z1[1], z1[2], z1[3]);
#pragma unroll
    for (int p = 0; p < 4; ++p) {
      v2 u0 = z0[p], u1 = z1[p];
      if (p) { u0 = ctw<-1>(u0, tw1a[p - 1]); u1 = ctw<-1>(u1, tw1b[p - 1]); }
      *reinterpret_cast<float4*>(buf + p * PR + n0) = make_float4(u0.x, u0.y, u1.x, u1.y);
    }
  }
  pow7(w2b, tw2);
  pow7(w3b, tw3);
  __syncthreads();
  v2* R = buf + wv * PR;
  v2 v[8];
#pragma unroll
  for (int b = 0; b < 8; ++b) v[b] = R[lane + 64 * b];
  fft8<-1>(v);
  __builtin_amdgcn_fence(__ATOMIC_SEQ_CST, "wavefront");
#pragma unroll
  for (int r = 0; r < 8; ++r) R[r * 72 + lane] = r ? ctw<-1>(v[r], tw2[r - 1]) : v[r];
  __builtin_amdgcn_fence(__ATOMIC_SEQ_CST, "wavefront");
#pragma unroll
  for (int e = 0; e < 8; ++e) v[e] = R[r3 * 72 + c3 + 8 * e];
  fft8<-1>(v);
  __builtin_amdgcn_fence(__ATOMIC_SEQ_CST, "wavefront");
#pragma unroll
  for (int u = 0; u < 8; ++u) R[r3 * 72 + c3 * 9 + u] = u ? ctw<-1>(v[u], tw3[u - 1]) : v[u];
  __builtin_amdgcn_fence(__ATOMIC_SEQ_CST, "wavefront");
#pragma unroll
  for (int c = 0; c < 8; ++c) v[c] = R[r3 * 72 + c * 9 + c3];
  fft8<-1>(v);
  {
    const unsigned w[8] = {wq0.x, wq0.y, wq0.z, wq0.w, wq1.x, wq1.y, wq1.z, wq1.w};
    constexpr float sc = 1.0f / ((float)S_ * (float)D_);
#pragma unroll
    for (int i = 0; i < 8; ++i) {
      const float a = bf2f((u16)(w[i] & 0xffff)) * sc, b = bf2f((u16)(w[i] >> 16)) * sc;
      const v2 qq = v[i];
      v[i] = {qq.x * a - qq.y * b, qq.x * b + qq.y * a};
    }
  }
  fft8<1>(v);
  __builtin_amdgcn_fence(__ATOMIC_SEQ_CST, "wavefront");
#pragma unroll
  for (int c = 0; c < 8; ++c) R[r3 * 72 + c * 9 + c3] = v[c];
  __builtin_amdgcn_fence(__ATOMIC_SEQ_CST, "wavefront");
#pragma unroll
  for (int u = 0; u < 8; ++u) { v2 qq = R[r3 * 72 + c3 * 9 + u]; v[u] = u ? ctw<1>(qq, tw3[u - 1]) : qq; }
  fft8<1>(v);
  __builtin_amdgcn_fence(__ATOMIC_SEQ_CST, "wavefront");
#pragma unroll
  for (int e = 0; e < 8; ++e) R[r3 * 72 + c3 + 8 * e] = v[e];
  __builtin_amdgcn_fence(__ATOMIC_SEQ_CST, "wavefront");
#pragma unroll
  for (int r = 0; r < 8; ++r) { v2 qq = R[r * 72 + lane]; v[r] = r ? ctw<1>(qq, tw2[r - 1]) : qq; }
  fft8<1>(v);
  __builtin_amdgcn_fence(__ATOMIC_SEQ_CST, "wavefront");
#pragma unroll
  for (int b = 0; b < 8; ++b) R[lane + 64 * b] = v[b];
  __syncthreads();
  {
    v2 z0[4], z1[4];
#pragma unroll
    for (int p = 0; p < 4; ++p) {
      float4 qq = *reinterpret_cast<const float4*>(buf + p * PR + n0);
      v2 u0 = {qq.x, qq.y}, u1 = {qq.z, qq.w};
      if (p) { u0 = ctw<1>(u0, tw1a[p - 1]); u1 = ctw<1>(u1, tw1b[p - 1]); }
      z0[p] = u0; z1[p] = u1;
    }
    fft4<1>(z0[0], z0[1], z0[2], z0[3]);
    fft4<1>(z1[0], z1[1], z1[2], z1[3]);
#pragma unroll
    for (int i = 0; i < 4; ++i) Tc[t + 256 * i] = make_float4(z0[i].x, z0[i].y, z1[i].x, z1[i].y);
  }
}

__global__ __launch_bounds__(512) void k_row_inv(const v2* __restrict__ T, const u16* __restrict__ x,
                                                 u16* __restrict__ x1, u16* __restrict__ uf,
                                                 const u16* __restrict__ wsrc, uint4* __restrict__ wdst) {
  __shared__ __attribute__((aligned(16))) v2 buf[8 * RS + 321 * 10];
  if (blockIdx.x >= S_ / 8) {
    const int half = threadIdx.x >> 8;
    prep_frag((blockIdx.x - S_ / 8) * 2 + half, threadIdx.x & 255, reinterpret_cast<u16*>(buf) + half * 32 * 72, wsrc, wdst, F_, D_);
    return;
  }
  v2* XT = buf + 8 * RS;
  const int t = threadIdx.x, wv = t >> 6, lane = t & 63;
  const int row0 = ((blockIdx.x & 7) * 32 + (blockIdx.x >> 3)) * 8;
  const int pB = lane >> 3, aB = lane & 7;
  const int rC = lane / 5;
  f32x4 tin[3];
#pragma unroll
  for (int i = 0; i < 3; ++i) {
    const int idx = min(t + 512 * i, DF_ * 4 - 1);
    tin[i] = *reinterpret_cast<const f32x4*>(T + (size_t)(idx >> 2) * S_ + row0 + 2 * (idx & 3));
  }
  v2 wP[5];
#pragma unroll
  for (int i = 0; i < 5; ++i) wP[i] = tw640(lane + 64 * i);
  unsigned raw[5];
  const unsigned* px = reinterpret_cast<const unsigned*>(x + (size_t)(row0 + wv) * D_) + lane;
#pragma unroll
  for (int i = 0; i < 5; ++i) raw[i] = px[64 * i];
  v2 wA = tw640(2 * lane);
  v2 wC = tw640(10 * (rC & 7));
#pragma unroll
  for (int i = 0; i < 3; ++i) {
    const int idx = t + 512 * i;
    if (idx < DF_ * 4) *reinterpret_cast<f32x4*>(XT + (idx >> 2) * 10 + 2 * (idx & 3)) = tin[i];
  }
  PIN2(wA); PIN2(wC);
#pragma unroll
  for (int i = 0; i < 5; ++i) { asm volatile("" : "+v"(raw[i])); PIN2(wP[i]); }
  __syncthreads();
  {
    v2* Z = buf + wv * RS;
#pragma unroll
    for (int i = 0; i < 5; ++i) {
      const int k = lane + 64 * i;
      v2 a = XT[k * 10 + wv], b = XT[(320 - k) * 10 + wv];
      b.y = -b.y;
      if (k == 0) { a.y = 0.f; b.y = 0.f; }
      const v2 d = ctw<1>((a - b), wP[i]);
      Z[k] = v2{a.x + b.x - d.y, a.y + b.y + d.x};
    }
  }
  __builtin_amdgcn_fence(__ATOMIC_SEQ_CST, "wavefront");
  v2* R = buf + wv * RS;
  if (lane < 40) {
    v2 u[8], tw[7];
#pragma unroll
    for (int tt = 0; tt < 8; ++tt) u[tt] = R[lane + 40 * tt];
    pow7(wC, tw);
    fft8<1>(u);
    __builtin_amdgcn_fence(__ATOMIC_SEQ_CST, "wavefront");
#pragma unroll
    for (int a = 0; a < 8; ++a) R[lane * 9 + a] = a ? ctw<1>(u[a], tw[a - 1]) : u[a];
    __builtin_amdgcn_fence(__ATOMIC_SEQ_CST, "wavefront");
#pragma unroll
    for (int r = 0; r < 8; ++r) u[r] = R[(r * 5 + pB) * 9 + aB];
    fft8<1>(u);
    __builtin_amdgcn_fence(__ATOMIC_SEQ_CST, "wavefront");
#pragma unroll
    for (int b = 0; b < 8; ++b) R[pB * 72 + aB + 8 * b] = u[b];
  }
  __builtin_amdgcn_fence(__ATOMIC_SEQ_CST, "wavefront");
  {
    v2 z[5];
    const v2 w2 = cmulf(wA, wA), w3 = cmulf(w2, wA), w4 = cmulf(w2, w2);
    z[0] = R[lane];
    z[1] = ctw<1>(R[72 + lane], wA);
    z[2] = ctw<1>(R[144 + lane], w2);
    z[3] = ctw<1>(R[216 + lane], w3);
    z[4] = ctw<1>(R[288 + lane], w4);
    fft5<1>(z);
    float v[10];
#pragma unroll
    for (int i = 0; i < 5; ++i) {
      v[2 * i] = bfr(bf2f((u16)(raw[i] & 0xffff)) + bfr(z[i].x));
      v[2 * i + 1] = bfr(bf2f((u16)(raw[i] >> 16)) + bfr(z[i].y));
    }
    const int row = row0 + wv;
    unsigned* o1 = reinterpret_cast<unsigned*>(x1 + (size_t)row * D_) + lane;
#pragma unroll
    for (int i = 0; i < 5; ++i) o1[64 * i] = pack2(v[2 * i], v[2 * i + 1]);
    layernorm10(v);
#pragma unroll
    for (int i = 0; i < 5; ++i) {
      const int d = 128 * i + 2 * lane;
      const size_t off = ((size_t)((row >> 4) * KTD + (d >> 5)) * 64 + ((d >> 3) & 3) * 16 + (row & 15)) * 8 + (d & 7);
      *reinterpret_cast<unsigned*>(uf + off) = pack2(v[2 * i], v[2 * i + 1]);
    }
  }
}

__device__ __forceinline__ float gelu_tanh(float x) {
  const float y2 = x * (2.302208198f + 0.1029432397f * x * x);
  const float e = __builtin_amdgcn_exp2f(y2);
  return x - x * __builtin_amdgcn_rcpf(e + 1.0f);
}

constexpr int G1_NS = 10;
#define WAITVM(n) asm volatile("s_waitcnt vmcnt(" #n ") lgkmcnt(0)" ::: "memory")
__global__ __launch_bounds__(512, 1) void k_gemm1(const uint4* __restrict__ w1f, const uint4* __restrict__ uf,
                                                  const u16* __restrict__ b1, u16* __restrict__ hf) {
  extern __shared__ __attribute__((aligned(16))) char smem[];
  constexpr int KT = D_ / 32;
  constexpr int NS = G1_NS;
  constexpr int NR = KT / 2;
  const int bid = blockIdx.x;
  const int xcd = bid & 7, loc = bid >> 3;
  const int mt = xcd * 2 + (loc & 1), ft = loc >> 1;
  const int wv = __builtin_amdgcn_readfirstlane(threadIdx.x >> 6), lane = threadIdx.x & 63;
  const int wf = wv >> 2, wm = wv & 3;
  const int f0 = ft * 128 + wf * 64, m0 = mt * 128 + wm * 32;
  u32x2 bb[4];
#pragma unroll
  for (int i = 0; i < 4; ++i) bb[i] = ld8_early(b1 + min(f0 + 16 * i + (lane >> 4) * 4, F_ - 4));
  const int ta = min(ft * 8 + wv, F_ / 16 - 1);
  const uint4* ga = w1f + (size_t)ta * KTD * 64 + lane;
  const uint4* gb = uf + (size_t)(mt * 8 + wv) * KTD * 64 + lane;
  char* lbase = smem + wv * 1024 + lane * 16;
#define G1_ISSUE(kt_) do { char* l_ = lbase + ((kt_) % NS) * 16384;                                  \
    __builtin_amdgcn_global_load_lds((const void*)(ga + (kt_) * 64), (void*)(l_), 16, 0, 0);          \
    __builtin_amdgcn_global_load_lds((const void*)(gb + (kt_) * 64), (void*)(l_ + 8192), 16, 0, 0); } while (0)
#pragma unroll
  for (int s = 0; s < NS; ++s) G1_ISSUE(s);
  f32x4 acc[4][2];
#pragma unroll
  for (int i = 0; i < 4; ++i) { acc[i][0] = f32x4{0.f, 0.f, 0.f, 0.f}; acc[i][1] = f32x4{0.f, 0.f, 0.f, 0.f}; }
  const char* ra = smem + (wf * 4) * 1024 + lane * 16;
  const char* rb = smem + (8 + wm * 2) * 1024 + lane * 16;
  bf16x8 A[2][2][4], B[2][2][2];
  WAITVM(16);
  __builtin_amdgcn_s_barrier();
  __builtin_amdgcn_sched_barrier(0);
#pragma unroll
  for (int h = 0; h < 2; ++h) {
#pragma unroll
    for (int i = 0; i < 4; ++i) A[0][h][i] = *reinterpret_cast<const bf16x8*>(ra + h * 16384 + i * 1024);
    B[0][h][0] = *reinterpret_cast<const bf16x8*>(rb + h * 16384);
    B[0][h][1] = *reinterpret_cast<const bf16x8*>(rb + h * 16384 + 1024);
  }
#pragma unroll
  for (int r = 0; r < NR; ++r) {
    if (r + 1 < NR) {
      const int lastR = (r + 3 > 4 ? r + 3 : 4) < NR - 1 ? (r + 3 > 4 ? r + 3 : 4) : NR - 1;
      const int nout = 4 * (lastR - (r + 1));
      switch (nout) {
        case 0: WAITVM(0); break;
        case 4: WAITVM(4); break;
        case 8: WAITVM(8); break;
        case 12: WAITVM(12); break;
        default: WAITVM(16); break;
      }
      __builtin_amdgcn_s_barrier();
      __builtin_amdgcn_sched_barrier(0);
      if (r >= 1 && r + 4 < NR) { G1_ISSUE(2 * (r + 4)); G1_ISSUE(2 * (r + 4) + 1); }
      const int so = ((2 * (r + 1)) % NS) * 16384;
#pragma unroll
      for (int h = 0; h < 2; ++h) {
#pragma unroll
        for (int i = 0; i < 4; ++i) A[(r + 1) & 1][h][i] = *reinterpret_cast<const bf16x8*>(ra + so + h * 16384 + i * 1024);
        B[(r + 1) & 1][h][0] = *reinterpret_cast<const bf16x8*>(rb + so + h * 16384);
        B[(r + 1) & 1][h][1] = *reinterpret_cast<const bf16x8*>(rb + so + h * 16384 + 1024);
      }
    }
#pragma unroll
    for (int h = 0; h < 2; ++h)
#pragma unroll
      for (int i = 0; i < 4; ++i)
#pragma unroll
        for (int j = 0; j < 2; ++j)
          acc[i][j] = __builtin_amdgcn_mfma_f32_16x16x32_bf16(A[r & 1][h][i], B[r & 1][h][j], acc[i][j], 0, 0, 0);
    __builtin_amdgcn_sched_barrier(0);
  }
#undef G1_ISSUE
  if (f0 >= F_) return;
#pragma unroll
  for (int i = 0; i < 4; ++i) {
    const int f = f0 + 16 * i + (lane >> 4) * 4;
    const float bv[4] = {bf2f((u16)(bb[i].x & 0xffff)), bf2f((u16)(bb[i].x >> 16)), bf2f((u16)(bb[i].y & 0xffff)), bf2f((u16)(bb[i].y >> 16))};
#pragma unroll
    for (int j = 0; j < 2; ++j) {
      const int m = m0 + 16 * j + (lane & 15);
      float h[4];
#pragma unroll
      for (int r = 0; r < 4; ++r) h[r] = gelu_tanh(bfr(bfr(acc[i][j][r]) + bv[r]));
      const size_t off = ((size_t)((m >> 4) * KTF + (f >> 5)) * 64 + ((f >> 3) & 3) * 16 + (m & 15)) * 8 + (f & 7);
      *reinterpret_cast<uint2*>(hf + off) = make_uint2(pack2(h[0], h[1]), pack2(h[2], h[3]));
    }
  }
}

constexpr int G2_ST = 3;
__global__ __launch_bounds__(512, 1) void k_gemm2(const bf16x8* __restrict__ w2f, const bf16x8* __restrict__ hf,
                                                  const u16* __restrict__ b2, const u16* __restrict__ x1,
                                                  u16* __restrict__ x2) {
  constexpr int ST = G2_ST;
  extern __shared__ __attribute__((aligned(16))) char smem[];
  f32x4* red = reinterpret_cast<f32x4*>(smem);
  const int bid = blockIdx.x;
  const int xcd = bid & 7, loc = bid >> 3;
  const int mt = xcd * 4 + (loc & 3), nt = loc >> 2;
  const int wv = __builtin_amdgcn_readfirstlane(threadIdx.x >> 6), lane = threadIdx.x & 63;
  const int n0 = nt * 80, m0 = mt * 64;
  const int k0 = wv < 6 ? wv * 7 : 42 + (wv - 6) * 6;
  const int kn = wv < 6 ? 7 : 6;
  const bf16x8* pa = w2f + ((size_t)(n0 >> 4) * KTF + k0) * 64 + lane;
  const bf16x8* pb = hf + ((size_t)(m0 >> 4) * KTF + k0) * 64 + lane;
  u32x2 bb[3], xr[3];
#pragma unroll
  for (int q = 0; q < 3; ++q) {
    const int tl = min(wv + 8 * q, 19);
    const int i = tl % 5, j = tl / 5;
    const int n = n0 + 16 * i + (lane >> 4) * 4, m = m0 + 16 * j + (lane & 15);
    bb[q] = ld8_early(b2 + n);
    xr[q] = ld8_early(x1 + (size_t)m * D_ + n);
  }
  f32x4 acc[5][4];
#pragma unroll
  for (int i = 0; i < 5; ++i)
#pragma unroll
    for (int j = 0; j < 4; ++j) acc[i][j] = f32x4{0.f, 0.f, 0.f, 0.f};
  bf16x8 A[ST][5], B[ST][4];
#pragma unroll
  for (int s = 0; s < ST - 1; ++s) {
#pragma unroll
    for (int i = 0; i < 5; ++i) A[s][i] = pa[(i * KTF + s) * 64];
#pragma unroll
    for (int j = 0; j < 4; ++j) B[s][j] = pb[(j * KTF + s) * 64];
  }
  __builtin_amdgcn_sched_barrier(0);
#pragma unroll
  for (int kt = 0; kt < 7; ++kt) {
    const int nx = kt + ST - 1;
    if (nx < 6 || (nx == 6 && kn == 7)) {
#pragma unroll
      for (int i = 0; i < 5; ++i) A[nx % ST][i] = pa[(i * KTF + nx) * 64];
#pragma unroll
      for (int j = 0; j < 4; ++j) B[nx % ST][j] = pb[(j * KTF + nx) * 64];
    }
    if (kt < 6 || kn == 7) {
#pragma unroll
      for (int i = 0; i < 5; ++i)
#pragma unroll
        for (int j = 0; j < 4; ++j)
          acc[i][j] = __builtin_amdgcn_mfma_f32_16x16x32_bf16(A[kt % ST][i], B[kt % ST][j], acc[i][j], 0, 0, 0);
    }
    __builtin_amdgcn_sched_barrier(0);
  }
#pragma unroll
  for (int j = 0; j < 4; ++j)
#pragma unroll
    for (int i = 0; i < 5; ++i) red[(wv * 20 + j * 5 + i) * 64 + lane] = acc[i][j];
  __syncthreads();
#pragma unroll
  for (int q = 0; q < 3; ++q) {
    const int tl = wv + 8 * q;
    if (tl < 20) {
      f32x4 s = red[tl * 64 + lane];
#pragma unroll
      for (int w = 1; w < 8; ++w) s += red[(w * 20 + tl) * 64 + lane];
      const int i = tl % 5, j = tl / 5;
      const int n = n0 + 16 * i + (lane >> 4) * 4, m = m0 + 16 * j + (lane & 15);
      const float bv[4] = {bf2f((u16)(bb[q].x & 0xffff)), bf2f((u16)(bb[q].x >> 16)), bf2f((u16)(bb[q].y & 0xffff)), bf2f((u16)(bb[q].y >> 16))};
      const float xv[4] = {bf2f((u16)(xr[q].x & 0xffff)), bf2f((u16)(xr[q].x >> 16)), bf2f((u16)(xr[q].y & 0xffff)), bf2f((u16)(xr[q].y >> 16))};
      float o[4];
#pragma unroll
      for (int r = 0; r < 4; ++r) o[r] = xv[r] + bfr(bfr(s[r]) + bv[r]);
      *reinterpret_cast<uint2*>(x2 + (size_t)m * D_ + n) = make_uint2(pack2(o[0], o[1]), pack2(o[2], o[3]));
    }
  }
}

extern "C" void kernel_launch(void* const* d_in, const int* in_sizes, int n_in,
                              void* d_out, int out_size, void* d_ws, size_t ws_size,
                              hipStream_t stream) {
  const u16* x0 = (const u16*)d_in[0];
  const u16* wr = (const u16*)d_in[1];
  const u16* wi = (const u16*)d_in[2];
  const u16* w1 = (const u16*)d_in[4];
  const u16* b1 = (const u16*)d_in[5];
  const u16* w2 = (const u16*)d_in[6];
  const u16* b2 = (const u16*)d_in[7];
  u16* out = (u16*)d_out;

  char* ws = (char*)d_ws;
  size_t off = 0;
  auto carve = [&](size_t bytes) { char* p = ws + off; off += (bytes + 255) & ~(size_t)255; return p; };
  float4* T = (float4*)carve((size_t)DF_ * S_ * 8);
  uint4* Wp = (uint4*)carve((size_t)DF_ * 256 * 32);
  uint4* w1f = (uint4*)carve((size_t)(F_ / 16) * KTD * 1024);
  uint4* w2f = (uint4*)carve((size_t)(D_ / 16) * KTF * 1024);
  u16* xa = (u16*)carve((size_t)S_ * D_ * 2);
  u16* xb = (u16*)carve((size_t)S_ * D_ * 2);
  u16* uf = (u16*)carve((size_t)(S_ / 16) * KTD * 1024);
  u16* hf = (u16*)carve((size_t)(S_ / 16) * KTF * 1024);

  const int nWp = (DF_ * 256 + 511) / 512;
  const int nW1 = (D_ / 32) * (F_ / 64);
  const int nW2 = ((F_ / 32) * (D_ / 64) + 1) / 2;
  constexpr int g1lds = G1_NS * 16384;
  hipFuncSetAttribute((const void*)k_gemm1, hipFuncAttributeMaxDynamicSharedMemorySize, g1lds);
  hipFuncSetAttribute((const void*)k_gemm2, hipFuncAttributeMaxDynamicSharedMemorySize, 163840);

  for (int layer = 0; layer < 2; ++layer) {
    const u16* xin = layer ? xb : x0;
    u16* xo = layer ? out : xb;
    k_row_fwd<<<S_ / 8 + (layer ? 0 : nWp), 512, 0, stream>>>(xin, (v2*)T, wr, wi, layer ? nullptr : Wp);
    k_col<<<DF_ + (layer ? 0 : nW1), 256, 0, stream>>>(T, Wp, w1, w1f);
    k_row_inv<<<S_ / 8 + (layer ? 0 : nW2), 512, 0, stream>>>((const v2*)T, xin, xa, uf, w2, w2f);
    k_gemm1<<<224, 512, g1lds, stream>>>(w1f, (const uint4*)uf, b1, hf);
    k_gemm2<<<256, 512, 163840, stream>>>((const bf16x8*)w2f, (const bf16x8*)hf, b2, xa, xo);
  }
}
```
